```python
import torch

def _softmax(x, axis=-1):
    return torch.softmax(x, dim=axis)

def _gelu(x):
    return torch.nn.functional.gelu(x, approximate='tanh')

def _silu(x):
    return torch.nn.functional.silu(x)

def _sigmoid(x):
    return torch.sigmoid(x)

def _layernorm(x, axis=-1, eps=1e-05):
    m = x.mean(dim=axis, keepdim=True)
    v = x.var(dim=axis, keepdim=True, unbiased=False)
    return (x - m) / torch.sqrt(v + eps)

def _scatter_add(tgt, idx, src, axis=0):
    return tgt.clone().index_add_(axis, idx, src)

def _segment_sum(src, seg, n):
    out = torch.zeros((n,) + src.shape[1:], dtype=src.dtype)
    return out.index_add_(0, seg, src)

def _pad(x, pw):
    flat = tuple((v for pair in reversed(pw) for v in pair))
    return torch.nn.functional.pad(x, flat)
kernel_name = 'parallel_sum__memory_aug_recurrent_mem_tr__dense_activation_silu__prompt'

def setup_inputs():

    _w = lambda a: (lambda t: t.to(torch.bfloat16) if t.is_floating_point() else t)(torch.as_tensor(a))

    def _A_setup():
        B, S, Dm = (1, 2048, 512)
        H, Dh = (32, 128)
        M, seg = (8, 32)
        seg = min(seg, S)
        S = S // seg * seg
        return {'x': _w((lambda *s: torch.randn(*s, dtype=torch.bfloat16) * 0.05)(B, S, Dm)), 'mem0': _w((lambda *s: torch.randn(*s, dtype=torch.bfloat16) * 0.4)(B, M, Dm)), 'Wq': _w((lambda *s: torch.randn(*s, dtype=torch.bfloat16) * 3.2)(Dm, H, Dh) * Dm ** (-0.5)), 'Wk': _w((lambda *s: torch.randn(*s, dtype=torch.bfloat16) * 3.2)(Dm, H, Dh) * Dm ** (-0.5)), 'Wv': _w((lambda *s: torch.randn(*s, dtype=torch.bfloat16) * 0.4)(Dm, H, Dh) * Dm ** (-0.5)), 'Wo': _w((lambda *s: torch.randn(*s, dtype=torch.bfloat16) * 0.4)(H, Dh, Dm) * (H * Dh) ** (-0.5)), 'seg_len': seg}
    _A_in = _A_setup()

    def _B_setup():
        B, S, D, F = (1, 2048, 512, 1792)
        return {'x': _w((lambda *s: torch.randn(*s, dtype=torch.bfloat16) * 0.05)(B, S, D)), 'w1': _w((lambda *s: torch.randn(*s, dtype=torch.bfloat16) * 1.6)(D, F) * D ** (-0.5)), 'w2': _w((lambda *s: torch.randn(*s, dtype=torch.bfloat16) * 0.1)(F, D) * F ** (-0.5))}
    _B_in = _B_setup()
    return {'A_x': _A_in['x'], 'A_mem0': _A_in['mem0'], 'A_Wq': _A_in['Wq'], 'A_Wk': _A_in['Wk'], 'A_Wv': _A_in['Wv'], 'A_Wo': _A_in['Wo'], 'A_seg_len': _A_in['seg_len'], 'B_x': _B_in['x'], 'B_w1': _B_in['w1'], 'B_w2': _B_in['w2']}

def reference(A_x, A_mem0, A_Wq, A_Wk, A_Wv, A_Wo, A_seg_len, B_x, B_w1, B_w2):

    def _A_ref(x, mem0, Wq, Wk, Wv, Wo, seg_len):
        B, S, Dm = x.shape
        H, Dh = (Wq.shape[1], Wq.shape[2])
        M = mem0.shape[1]
        scale = Dh ** (-0.5)
        n_seg = S // seg_len
        L = M + seg_len
        idx = torch.arange(L)
        mask = (idx[:, None] < M) | (idx[:, None] >= idx[None, :])
        mem = mem0
        outs = []
        for s in range(n_seg):
            xs = x[:, s * seg_len:(s + 1) * seg_len, :].clone()
            h = torch.concatenate([mem, xs], axis=1)
            q = torch.einsum('btd,dhe->bhte', h, Wq)
            k = torch.einsum('btd,dhe->bhte', h, Wk)
            v = torch.einsum('btd,dhe->bhte', h, Wv)
            scores = torch.einsum('bhqe,bhke->bhqk', q, k) * scale
            scores = torch.where(mask[None, None], scores, -1000000000.0)
            p = _softmax(scores, axis=-1)
            o = torch.einsum('bhqk,bhke->bhqe', p, v)
            o = torch.einsum('bhte,hed->btd', o, Wo)
            mem = o[:, :M, :].clone()
            outs.append(o[:, M:, :])
        return torch.concatenate(outs, axis=1)

    def _B_ref(x, w1, w2):
        h = torch.einsum('bsd,df->bsf', x, w1)
        h = _silu(h)
        out = torch.einsum('bsf,fd->bsd', h, w2)
        return out
    x = A_x
    xn = _layernorm(x, axis=-1)
    return x + _A_ref(xn, A_mem0, A_Wq, A_Wk, A_Wv, A_Wo, A_seg_len) + _B_ref(xn, B_w1, B_w2)
```

```cpp
#include <hip/hip_runtime.h>
#include <hip/hip_bf16.h>

typedef __bf16 bf16x2 __attribute__((ext_vector_type(2)));
typedef __bf16 bf16x8 __attribute__((ext_vector_type(8)));
typedef __bf16 bf16x16 __attribute__((ext_vector_type(16)));
typedef short s16x4 __attribute__((ext_vector_type(4)));
typedef float f32x2 __attribute__((ext_vector_type(2)));
typedef float f32x4 __attribute__((ext_vector_type(4)));
typedef unsigned u32x2 __attribute__((ext_vector_type(2)));
typedef unsigned u32x4 __attribute__((ext_vector_type(4)));
typedef unsigned u32x8 __attribute__((ext_vector_type(8)));
typedef __amdgpu_buffer_rsrc_t rsrc_t;
#define LAS __attribute__((address_space(3)))
#define DEV __device__ __forceinline__

constexpr unsigned OFF_INBOX = 0u;
constexpr unsigned OFF_MEMF = 2u << 20;
constexpr unsigned OFF_FLAG = 4u << 20;
constexpr unsigned FL_PROJ = OFF_FLAG;
constexpr unsigned FL_CAL = OFF_FLAG + 64 * 128;
constexpr unsigned FL_O = OFF_FLAG + 256 * 128;
constexpr unsigned FL_ABORT = OFF_FLAG + 128 * 128;
constexpr unsigned FL_XN = OFF_FLAG + 160 * 128;
constexpr unsigned FL_FILL = OFF_FLAG + 224 * 128;
constexpr unsigned FL_QUIET = OFF_FLAG + 384 * 128;
constexpr unsigned FL_REQ = OFF_FLAG + 416 * 128;
constexpr unsigned FL_ZERO_END = OFF_FLAG + 544 * 128;
constexpr unsigned FL_ACK = OFF_FLAG + 544 * 128;
constexpr unsigned FL_T0 = FL_CAL + 16;
constexpr int MASTER = 160;
constexpr unsigned OFF_XN = 6u << 20;
constexpr unsigned OFF_H = 8u << 20;
constexpr unsigned OFF_Q = 16u << 20;
constexpr unsigned OFF_K = 34u << 20;
constexpr unsigned OFF_VT = 52u << 20;
constexpr unsigned OFF_O = 70u << 20;
constexpr unsigned OFF_DBG = 88u << 20;
constexpr unsigned HSTRIDE = 524288u + 8192u;
constexpr unsigned WS_BYTES = 96u << 20;
constexpr unsigned SENT = 0xFFFFFFFFu;
constexpr int LDS_BYTES = 160 * 1024;
constexpr unsigned SPIN_CAP = 1u << 18;
constexpr int SC1 = 16;

constexpr int NSET = 4;
#define XCC_AND 7u
#define XCC_OFF 0
#define MA_SLEEP 12
#define OUT_SLEEP 12
#define PROJ_AUX 0
#define XF_AT 4
#define OUT_POLL 16
#define MA_POLL 3
#define QH_SLEEP 0
#define LAST_SLEEP 16

struct Params {
    const unsigned short* x; const unsigned short* mem0; const unsigned short* wq; const unsigned short* wk;
    const unsigned short* wv; const unsigned short* wo; const unsigned short* w1; const unsigned short* w2;
    unsigned short* out; unsigned char* ws;
};

DEV rsrc_t mk_rsrc(const void* p, unsigned bytes) { return __builtin_amdgcn_make_buffer_rsrc((void*)p, 0, bytes, 0x00020000); }
DEV unsigned pk(float a, float b) { f32x2 v = {a, b}; return __builtin_bit_cast(unsigned, __builtin_convertvector(v, bf16x2)); }
DEV float bfr(float a) { return __builtin_bit_cast(float, pk(a, a) & 0xffff0000u); }
DEV float bflo(unsigned u) { return __builtin_bit_cast(float, u << 16); }
DEV float bfhi(unsigned u) { return __builtin_bit_cast(float, u & 0xffff0000u); }
DEV unsigned plo(unsigned a, unsigned b) { return __builtin_amdgcn_perm(b, a, 0x05040100u); }
DEV unsigned phi(unsigned a, unsigned b) { return __builtin_amdgcn_perm(b, a, 0x07060302u); }
DEV f32x4 smf(u32x4 a, u32x8 b, f32x4 c, int idx) {
    return __builtin_amdgcn_smfmac_f32_16x16x64_bf16(__builtin_bit_cast(bf16x8, a), __builtin_bit_cast(bf16x16, b), c, idx, 0, 0); }
DEV f32x4 mf32(u32x4 a, u32x4 b, f32x4 c) {
    return __builtin_amdgcn_mfma_f32_16x16x32_bf16(__builtin_bit_cast(bf16x8, a), __builtin_bit_cast(bf16x8, b), c, 0, 0, 0); }
DEV f32x4 zacc() { f32x4 z = {0.f, 0.f, 0.f, 0.f}; asm volatile("" : "+v"(z)); return z; }
DEV u32x8 cat(u32x4 a, u32x4 b) { return __builtin_shufflevector(a, b, 0, 1, 2, 3, 4, 5, 6, 7); }
template <int N> DEV unsigned ror_u(unsigned v) { return (unsigned)__builtin_amdgcn_update_dpp(0, (int)v, 0x120 + N, 0xf, 0xf, false); }
template <int N> DEV float ror_f(float v) { return __builtin_bit_cast(float, ror_u<N>(__builtin_bit_cast(unsigned, v))); }
DEV float max_x16(float v) { const unsigned u = __builtin_bit_cast(unsigned, v); auto r = __builtin_amdgcn_permlane16_swap(u, u, false, false); const unsigned r0 = r[0], r1 = r[1];
    return fmaxf(__builtin_bit_cast(float, r0), __builtin_bit_cast(float, r1)); }
DEV float max_x32(float v) { const unsigned u = __builtin_bit_cast(unsigned, v); auto r = __builtin_amdgcn_permlane32_swap(u, u, false, false); const unsigned r0 = r[0], r1 = r[1];
    return fmaxf(__builtin_bit_cast(float, r0), __builtin_bit_cast(float, r1)); }
DEV float add_x16(float v) { const unsigned u = __builtin_bit_cast(unsigned, v); auto r = __builtin_amdgcn_permlane16_swap(u, u, false, false); const unsigned r0 = r[0], r1 = r[1];
    return __builtin_bit_cast(float, r0) + __builtin_bit_cast(float, r1); }
DEV float add_x32(float v) { const unsigned u = __builtin_bit_cast(unsigned, v); auto r = __builtin_amdgcn_permlane32_swap(u, u, false, false); const unsigned r0 = r[0], r1 = r[1];
    return __builtin_bit_cast(float, r0) + __builtin_bit_cast(float, r1); }
DEV unsigned umax4(u32x4 v) { return max(max(v.x, v.y), max(v.z, v.w)); }
DEV u32x4 ld16(rsrc_t r, unsigned voff, unsigned soff) { return __builtin_amdgcn_raw_buffer_load_b128(r, voff, soff, SC1); }
DEV void st16(u32x4 v, rsrc_t r, unsigned voff, unsigned soff) {
    __builtin_amdgcn_raw_buffer_store_b128(v, r, voff, soff, SC1); asm volatile("s_nop 1" :: "v"(v) : "memory"); }
DEV unsigned ldflag(rsrc_t r, unsigned voff) { asm volatile("" ::: "memory"); return __builtin_amdgcn_raw_buffer_load_b32(r, voff, 0, SC1); }
DEV void stflag(unsigned v, rsrc_t r, unsigned off) { __builtin_amdgcn_raw_buffer_store_b32(v, r, off, 0, SC1); }
#define WG_BARRIER() asm volatile("s_waitcnt lgkmcnt(0)\n\ts_barrier" ::: "memory")
#define DRAIN() asm volatile("s_waitcnt vmcnt(0)" ::: "memory")
#define FENCE() __builtin_amdgcn_sched_barrier(0)
DEV u32x4 lds_r16(LAS unsigned char* p) { return *(LAS u32x4*)p; }
DEV void lds_w16(LAS unsigned char* p, u32x4 v) { *(LAS u32x4*)p = v; }

DEV int lane_id() { int l; asm volatile("v_mbcnt_lo_u32_b32 %0, -1, 0\n\tv_mbcnt_hi_u32_b32 %0, -1, %0" : "=v"(l)); return l; }
DEV void spin_check(unsigned& spins, rsrc_t ws) {
    if (++spins > SPIN_CAP) { stflag(1u, ws, FL_ABORT); __builtin_amdgcn_endpgm(); }
    if ((spins & 255u) == 0u && __builtin_amdgcn_readfirstlane(ldflag(ws, FL_ABORT)) != 0u) __builtin_amdgcn_endpgm();
}
DEV void wait_flags(rsrc_t ws, unsigned voff, unsigned need, int sleep_far) {
    unsigned spins = 0;
    for (;;) {
        const unsigned v = ldflag(ws, voff);
        if (__all(v >= need)) return;
        if (sleep_far < 0) __builtin_amdgcn_s_sleep(OUT_POLL);
        else if (sleep_far && __any(v + 1u < need)) { for (int i = 0; i < sleep_far; ++i) __builtin_amdgcn_s_sleep(16); }
        else __builtin_amdgcn_s_sleep(2);
        spin_check(spins, ws);
    }
}

struct Hs { unsigned slot, req, bid; int w; bool again; };
DEV void hs_send(rsrc_t ws, Hs& hs, unsigned a0, unsigned xcc) {
    hs.req = ((((unsigned)__builtin_amdgcn_readfirstlane(a0) >> 4) + 1u) << 4) | 8u | xcc;
    if (!hs.again) stflag(hs.req, ws, FL_REQ + hs.slot);
}
DEV void hs_wait(rsrc_t ws, const Hs& hs) {
    if (hs.again) return;
    unsigned ack;
    for (unsigned spins = 0;; ++spins) {
        ack = (unsigned)__builtin_amdgcn_readfirstlane(ldflag(ws, FL_ACK + hs.slot));
        if ((ack | 8u) == hs.req) break;
        if (spins > SPIN_CAP) __builtin_amdgcn_endpgm();
        stflag(hs.req, ws, FL_REQ + hs.slot);
        __builtin_amdgcn_s_sleep(4);
    }
    if (!(ack & 8u)) {
        asm volatile("s_waitcnt vmcnt(0) lgkmcnt(0)\n\ts_mov_b64 exec, -1\n\tv_mbcnt_lo_u32_b32 v0, -1, 0\n\tv_mbcnt_hi_u32_b32 v0, -1, v0\n\tv_add_u32 v0, s3, v0\n\t"
                     "s_getpc_b64 s[4:5]\n1:\n\ts_add_u32 s4, s4, _Z6k_main6Params-1b\n\ts_addc_u32 s5, s5, -1\n\ts_setpc_b64 s[4:5]"
                     :: "{s[0:1]}"(__builtin_amdgcn_kernarg_segment_ptr()), "{s2}"(hs.bid | 0x10000u), "{s3}"(hs.w * 64) : "memory");
        __builtin_amdgcn_endpgm();
    }
}
DEV unsigned hs_master(rsrc_t ws, LAS unsigned char* lds, int w) {
    const unsigned lane = (unsigned)lane_id(), tid = 64u * (unsigned)w + lane;
    const u32x4 z = {0u, 0u, 0u, 0u};
#pragma unroll
    for (int i = 16; i >= 0; --i) st16(z, ws, tid * 16u, OFF_FLAG + (unsigned)i * 4096u);
    DRAIN();
    WG_BARRIER();
    if (tid == 0u) stflag((unsigned)__builtin_amdgcn_s_memrealtime(), ws, FL_T0);
    u32x4 rq;
    for (unsigned spins = 0;; ++spins) {
        asm volatile("" ::: "memory");
        rq = ld16(ws, tid * 64u, FL_REQ);
        if (tid == (unsigned)MASTER) { const unsigned own = 8u | (((unsigned)__builtin_amdgcn_s_getreg(20 | (3 << 11)) + XCC_OFF) & XCC_AND); rq = (u32x4){own, own, own, own}; }
        if (__all(min(min(rq[0], rq[1]), min(rq[2], rq[3])) != 0u)) break;
        if (spins > SPIN_CAP) __builtin_amdgcn_endpgm();
    }
    unsigned m = 1u << (rq[0] & 7u);
    m |= (unsigned)__shfl_xor((int)m, 1); m |= (unsigned)__shfl_xor((int)m, 2); m |= (unsigned)__shfl_xor((int)m, 4);
    const bool ok = __all(m == 0xFFu);
    LAS unsigned* L = (LAS unsigned*)lds;
    if (lane == 0u) L[w] = ok;
    DRAIN();
    WG_BARRIER();
    const unsigned keep = (L[0] & L[1] & L[2] & L[3]) ? ~0u : ~8u;
    if (tid != (unsigned)MASTER) st16(rq & keep, ws, tid * 64u, FL_ACK);
    return keep;
}

DEV void load_proj_frags(rsrc_t w, unsigned voff, int ks, u32x8& f0, u32x8& f1) {
    const unsigned sb = (unsigned)(128 * (ks >> 1) + 4 * (ks & 1)) * 8192u;
    unsigned L[16];
#pragma unroll
    for (int j = 0; j < 16; ++j) {
        const unsigned dc = 64 * (j >> 3) + 16 * ((j >> 1) & 1) + 2 * ((j >> 2) & 1) + (j & 1);
        L[j] = __builtin_amdgcn_raw_buffer_load_b32(w, voff, sb + dc * 8192u, 0);
    }
#pragma unroll
    for (int m = 0; m < 8; ++m) { f0[m] = plo(L[2 * m], L[2 * m + 1]); f1[m] = phi(L[2 * m], L[2 * m + 1]); }
}
DEV void lds_wfrag(LAS unsigned char* p, u32x8 f) {
    lds_w16(p, __builtin_shufflevector(f, f, 0, 1, 2, 3)); lds_w16(p + 1024, __builtin_shufflevector(f, f, 4, 5, 6, 7)); }
DEV u32x8 lds_rfrag(LAS unsigned char* p) { return cat(lds_r16(p), lds_r16(p + 1024)); }
#define TLS(k) do {} while (0)
#define PIN_A(x) asm volatile("" : "+a"(x))
#define PIN_V(x) asm volatile("" : "+v"(x))

typedef unsigned u32x16 __attribute__((ext_vector_type(16)));
DEV void role_R(const Params& p, rsrc_t ws, unsigned w, unsigned wg, unsigned bid, unsigned xcc) {
    const unsigned lane = (unsigned)lane_id(), c = lane & 15, g = lane >> 4, uh = 16u * (wg >> 5) + (wg & 15u), sg = (wg >> 4) & 1u;
    const unsigned kw = w;
    const unsigned jr = wg >> 1, wr = 2u * (wg & 1u) + w;
    TLS(0);
    rsrc_t rq = mk_rsrc(p.wq, 512u * 8192u), rk = mk_rsrc(p.wk, 512u * 8192u), rv = mk_rsrc(p.wv, 512u * 8192u), ro = mk_rsrc(p.wo, 4096u * 1024u);
    unsigned s_wl = 32768u + w * 32768u;
    const unsigned s_kvf = FL_PROJ + uh * 128u;
    const auto karg = __builtin_amdgcn_kernarg_segment_ptr();
    const unsigned ah = (c >> 1) & 1u;
    u32x16 vin0; u32x2 vin1; u32x8 vin2;
    vin0[0] = s_wl + lane * 16u;
    vin0[1] = lane * 16u + 2u * kw * 1024u;
    vin0[2] = lane * 16u + ((2u * kw) ^ 2u) * 1024u;
    vin0[3] = lane * 16u + ((2u * kw) ^ 4u) * 1024u;
    vin0[4] = lane * 16u + ((2u * kw) ^ 6u) * 1024u;
    vin0[5] = 8192u + w * 512u + (c >> 2) * 128u + ((2u * g) ^ (2u * (c >> 2))) * 16u + 4u * (c & 3u);
    vin0[6] = w * 512u + g * 128u + ((c & 7u) ^ (2u * g)) * 16u;
    vin0[7] = g * 128u + ((c & 7u) ^ (2u * g)) * 16u;
    vin0[8] = 10240u + (w >> 1) * 1024u + (16u * (2u * (w & 1u) + (c >> 3)) + 4u * g + 2u * (c & 1u)) * 16u + 4u * ((c & 7u) >> 1);
    vin0[9] = lane * 16u;
    vin0[10] = (w == 2u) ? lane * 32u : 0x7FFFFF00u;
    vin0[11] = (4u * w + g) * 32768u + (c >> 2) * 2048u + uh * 64u + (c & 3u) * 16u;
    vin0[12] = (w < 2u) ? (jr & 15u) * 32768u + wr * 2048u + lane * 16u : 0x7FFFFF00u;
    vin0[13] = (w < 2u && c < 8u && g < 2u) ? (c >> 2) * 16384u + (2u * ((jr >> 2) & 3u) + (jr >> 4)) * 1024u + wr * 256u + (jr & 3u) * 64u + ((c & 2u) + g) * 16u + (c & 1u) * 8u : 0x7FFFFF00u;
    vin0[14] = (w >> 1) * 1024u + (16u * (2u * (w & 1u) + (g >> 1)) + 4u * ((c & 7u) >> 1) + 2u * (g & 1u) + (c & 1u)) * 16u;
    vin0[15] = uh * HSTRIDE + ((w >> 1) * 4u + 2u * (c & 1u)) * 1024u + (16u * g + 8u * (w & 1u) + (c >> 1)) * 16u;
    vin1[0] = ah ? 0xEEEEu : 0x4444u;
    vin1[1] = (w == 3u && lane == 0u) ? wg * 32u : (w == 3u && lane == 1u && sg == 0u) ? 4096u + uh * 128u : 0x7FFFFF00u;
    {
        const unsigned rj = lane >> 5, rg = (lane >> 3) & 3u, pc = lane & 7u;
        vin2[0] = (rj + 32u * (rg >> 1) + 8u * (rg & 1u)) * 8192u + uh * 256u + (w >> 1) * 128u + ((pc >> 2) ^ (rg & 1u)) * 64u + (pc & 3u) * 16u;
        vin2[1] = (uh * 128u + 16u * (g >> 1) + 4u * (g & 1u)) * 1024u + (128u * w + 8u * c + 4u * sg) * 2u;
        const unsigned rd = g * 128u + ((w ^ g) & 1u) * 64u + 4u * c;
        vin2[2] = s_wl + rd;
        vin2[3] = w * 8192u + rd;
        const unsigned one = (c & 4u) ? 0x3F800000u : 0x00003F80u;
#pragma unroll
        for (unsigned m = 0; m < 4; ++m) vin2[4 + m] = (c < 8u && (c & 3u) == m) ? one : 0u;
    }
    asm volatile(
        "s_nop 4\n"
        "s_mov_b32 s64, 0x05040100\n"
        "s_mov_b32 s65, 0x07060302\n"
        "s_xor_b32 s66, s61, 0\n"
        "s_lshl_b32 s68, s66, 20\n"
        "s_xor_b32 s66, s61, 2\n"
        "s_lshl_b32 s70, s66, 20\n"
        "s_lshl_b32 s91, s61, 13\n"
        "s_and_b32 s66, s63, 255\n"
        "s_lshl_b32 s66, s66, 6\n"
        "s_lshl_b32 s67, s61, 2\n"
        "s_add_u32 s66, s66, s67\n"
        "s_add_u32 s74, s66, 4263936\n"
        "s_add_u32 s73, s66, 4247552\n"
        "s_cmp_gt_u32 s63, 255\n"
        "s_cselect_b32 s73, 0x7fffff00, s73\n"
        "buffer_load_dword v54, off, s[40:43], s74 sc1\n"
        "s_add_u32 s66, s68, 0\n"
        "s_add_u32 m0, s60, 16384\n"
        "s_nop 0\n"
        "buffer_load_dwordx4 v20, s[44:47], s66 offen lds\n"
        "s_add_u32 s66, s68, 131072\n"
        "s_add_u32 m0, s60, 17408\n"
        "s_nop 0\n"
        "buffer_load_dwordx4 v20, s[44:47], s66 offen lds\n"
        "s_add_u32 s66, s68, 16384\n"
        "s_add_u32 m0, s60, 18432\n"
        "s_nop 0\n"
        "buffer_load_dwordx4 v20, s[44:47], s66 offen lds\n"
        "s_add_u32 s66, s68, 147456\n"
        "s_add_u32 m0, s60, 19456\n"
        "s_nop 0\n"
        "buffer_load_dwordx4 v20, s[44:47], s66 offen lds\n"
        "s_add_u32 s66, s68, 524288\n"
        "s_add_u32 m0, s60, 20480\n"
        "s_nop 0\n"
        "buffer_load_dwordx4 v20, s[44:47], s66 offen lds\n"
        "s_add_u32 s66, s68, 655360\n"
        "s_add_u32 m0, s60, 21504\n"
        "s_nop 0\n"
        "buffer_load_dwordx4 v20, s[44:47], s66 offen lds\n"
        "s_add_u32 s66, s68, 540672\n"
        "s_add_u32 m0, s60, 22528\n"
        "s_nop 0\n"
        "buffer_load_dwordx4 v20, s[44:47], s66 offen lds\n"
        "s_add_u32 s66, s68, 671744\n"
        "s_add_u32 m0, s60, 23552\n"
        "s_nop 0\n"
        "buffer_load_dwordx4 v20, s[44:47], s66 offen lds\n"
        "s_add_u32 s66, s68, 32768\n"
        "s_add_u32 m0, s60, 24576\n"
        "s_nop 0\n"
        "buffer_load_dwordx4 v20, s[44:47], s66 offen lds\n"
        "s_add_u32 s66, s68, 163840\n"
        "s_add_u32 m0, s60, 25600\n"
        "s_nop 0\n"
        "buffer_load_dwordx4 v20, s[44:47], s66 offen lds\n"
        "s_add_u32 s66, s68, 49152\n"
        "s_add_u32 m0, s60, 26624\n"
        "s_nop 0\n"
        "buffer_load_dwordx4 v20, s[44:47], s66 offen lds\n"
        "s_add_u32 s66, s68, 180224\n"
        "s_add_u32 m0, s60, 27648\n"
        "s_nop 0\n"
        "buffer_load_dwordx4 v20, s[44:47], s66 offen lds\n"
        "s_add_u32 s66, s68, 557056\n"
        "s_add_u32 m0, s60, 28672\n"
        "s_nop 0\n"
        "buffer_load_dwordx4 v20, s[44:47], s66 offen lds\n"
        "s_add_u32 s66, s68, 688128\n"
        "s_add_u32 m0, s60, 29696\n"
        "s_nop 0\n"
        "buffer_load_dwordx4 v20, s[44:47], s66 offen lds\n"
        "s_add_u32 s66, s68, 573440\n"
        "s_add_u32 m0, s60, 30720\n"
        "s_nop 0\n"
        "buffer_load_dwordx4 v20, s[44:47], s66 offen lds\n"
        "s_add_u32 s66, s68, 704512\n"
        "s_add_u32 m0, s60, 31744\n"
        "s_nop 0\n"
        "buffer_load_dwordx4 v20, s[44:47], s66 offen lds\n"
        "s_waitcnt vmcnt(16)\n"
        "v_readfirstlane_b32 s72, v54\n"
        "s_nop 1\n"
        "s_lshr_b32 s72, s72, 4\n"
        "s_add_u32 s72, s72, 1\n"
        "s_lshl_b32 s72, s72, 4\n"
        "s_or_b32 s72, s72, 8\n"
        "s_or_b32 s72, s72, s97\n"
        "v_mov_b32 v55, s72\n"
        "buffer_store_dword v55, off, s[40:43], s73 sc1\n"
        "s_and_b32 s66, s61, 1\n"
        "s_lshl_b32 s66, s66, 16\n"
        "s_sub_u32 s66, 32768, s66\n"
        "v_add_u32 v52, s66, v22\n"
        "v_xor_b32 v53, 8192, v23\n"
        "s_waitcnt vmcnt(9)\n"
        "s_barrier\n"
        "ds_read2st64_b32 v[64:65], v22 offset0:64 offset1:66\n"
        "ds_read2st64_b32 v[66:67], v22 offset0:68 offset1:70\n"
        "ds_read2st64_b32 v[68:69], v22 offset0:72 offset1:74\n"
        "ds_read2st64_b32 v[70:71], v22 offset0:76 offset1:78\n"
        "ds_read2st64_b32 v[72:73], v22 offset0:80 offset1:82\n"
        "ds_read2st64_b32 v[74:75], v22 offset0:84 offset1:86\n"
        "ds_read2st64_b32 v[76:77], v22 offset0:88 offset1:90\n"
        "ds_read2st64_b32 v[78:79], v22 offset0:92 offset1:94\n"
        "ds_read2st64_b32 v[80:81], v52 offset0:64 offset1:66\n"
        "ds_read2st64_b32 v[82:83], v52 offset0:68 offset1:70\n"
        "ds_read2st64_b32 v[84:85], v52 offset0:72 offset1:74\n"
        "ds_read2st64_b32 v[86:87], v52 offset0:76 offset1:78\n"
        "ds_read2st64_b32 v[88:89], v52 offset0:80 offset1:82\n"
        "ds_read2st64_b32 v[90:91], v52 offset0:84 offset1:86\n"
        "ds_read2st64_b32 v[92:93], v52 offset0:88 offset1:90\n"
        "ds_read2st64_b32 v[94:95], v52 offset0:92 offset1:94\n"
        "s_add_u32 s66, s68, 0\n"
        "s_add_u32 m0, s91, 0\n"
        "s_nop 0\n"
        "buffer_load_dwordx4 v20, s[48:51], s66 offen lds\n"
        "s_mov_b32 s66, 0\n"
        "buffer_load_dwordx2 v[192:193], v21, s[56:59], s66 offen\n"
        "s_add_u32 s66, s68, 131072\n"
        "s_add_u32 m0, s91, 1024\n"
        "s_nop 0\n"
        "buffer_load_dwordx4 v20, s[48:51], s66 offen lds\n"
        "s_mov_b32 s66, 1024\n"
        "buffer_load_dwordx2 v[194:195], v21, s[56:59], s66 offen\n"
        "buffer_store_dword v55, off, s[40:43], s73 sc1\n"
        "s_waitcnt lgkmcnt(8)\n"
        "v_perm_b32 v36, v65, v64, s64\n"
        "v_perm_b32 v37, v67, v66, s64\n"
        "v_perm_b32 v38, v69, v68, s64\n"
        "v_perm_b32 v39, v71, v70, s64\n"
        "s_add_u32 s66, s68, 16384\n"
        "s_add_u32 m0, s91, 2048\n"
        "s_nop 0\n"
        "buffer_load_dwordx4 v20, s[48:51], s66 offen lds\n"
        "s_mov_b32 s66, 8192\n"
        "buffer_load_dwordx2 v[196:197], v21, s[56:59], s66 offen\n"
        "v_perm_b32 v40, v73, v72, s64\n"
        "v_perm_b32 v41, v75, v74, s64\n"
        "v_perm_b32 v42, v77, v76, s64\n"
        "v_perm_b32 v43, v79, v78, s64\n"
        "s_add_u32 s66, s68, 147456\n"
        "s_add_u32 m0, s91, 3072\n"
        "s_nop 0\n"
        "buffer_load_dwordx4 v20, s[48:51], s66 offen lds\n"
        "s_mov_b32 s66, 9216\n"
        "buffer_load_dwordx2 v[198:199], v21, s[56:59], s66 offen\n"
        "v_perm_b32 v44, v65, v64, s65\n"
        "v_perm_b32 v45, v67, v66, s65\n"
        "v_perm_b32 v46, v69, v68, s65\n"
        "v_perm_b32 v47, v71, v70, s65\n"
        "s_add_u32 s66, s68, 524288\n"
        "s_add_u32 m0, s91, 4096\n"
        "s_nop 0\n"
        "buffer_load_dwordx4 v20, s[48:51], s66 offen lds\n"
        "s_mov_b32 s66, 2048\n"
        "buffer_load_dwordx2 v[200:201], v21, s[56:59], s66 offen\n"
        "v_perm_b32 v48, v73, v72, s65\n"
        "v_perm_b32 v49, v75, v74, s65\n"
        "v_perm_b32 v50, v77, v76, s65\n"
        "v_perm_b32 v51, v79, v78, s65\n"
        "s_add_u32 s66, s68, 655360\n"
        "s_add_u32 m0, s91, 5120\n"
        "s_nop 0\n"
        "buffer_load_dwordx4 v20, s[48:51], s66 offen lds\n"
        "s_mov_b32 s66, 3072\n"
        "buffer_load_dwordx2 v[202:203], v21, s[56:59], s66 offen\n"
        "ds_write_b128 v0, v[36:39] offset:0\n"
        "ds_write_b128 v0, v[40:43] offset:1024\n"
        "ds_write_b128 v0, v[44:47] offset:2048\n"
        "ds_write_b128 v0, v[48:51] offset:3072\n"
        "s_waitcnt lgkmcnt(4)\n"
        "v_perm_b32 v28, v81, v80, s64\n"
        "v_accvgpr_write_b32 a64, v28\n"
        "v_perm_b32 v29, v83, v82, s64\n"
        "v_accvgpr_write_b32 a65, v29\n"
        "v_perm_b32 v30, v85, v84, s64\n"
        "v_accvgpr_write_b32 a66, v30\n"
        "v_perm_b32 v31, v87, v86, s64\n"
        "v_accvgpr_write_b32 a67, v31\n"
        "s_add_u32 s66, s68, 540672\n"
        "s_add_u32 m0, s91, 6144\n"
        "s_nop 0\n"
        "buffer_load_dwordx4 v20, s[48:51], s66 offen lds\n"
        "s_mov_b32 s66, 10240\n"
        "buffer_load_dwordx2 v[204:205], v21, s[56:59], s66 offen\n"
        "v_perm_b32 v32, v89, v88, s64\n"
        "v_accvgpr_write_b32 a68, v32\n"
        "v_perm_b32 v33, v91, v90, s64\n"
        "v_accvgpr_write_b32 a69, v33\n"
        "v_perm_b32 v34, v93, v92, s64\n"
        "v_accvgpr_write_b32 a70, v34\n"
        "v_perm_b32 v35, v95, v94, s64\n"
        "v_accvgpr_write_b32 a71, v35\n"
        "s_add_u32 s66, s68, 671744\n"
        "s_add_u32 m0, s91, 7168\n"
        "s_nop 0\n"
        "buffer_load_dwordx4 v20, s[48:51], s66 offen lds\n"
        "s_mov_b32 s66, 11264\n"
        "buffer_load_dwordx2 v[206:207], v21, s[56:59], s66 offen\n"
        "v_perm_b32 v28, v81, v80, s65\n"
        "v_accvgpr_write_b32 a72, v28\n"
        "v_perm_b32 v29, v83, v82, s65\n"
        "v_accvgpr_write_b32 a73, v29\n"
        "v_perm_b32 v30, v85, v84, s65\n"
        "v_accvgpr_write_b32 a74, v30\n"
        "v_perm_b32 v31, v87, v86, s65\n"
        "v_accvgpr_write_b32 a75, v31\n"
        "v_perm_b32 v32, v89, v88, s65\n"
        "v_accvgpr_write_b32 a76, v32\n"
        "v_perm_b32 v33, v91, v90, s65\n"
        "v_accvgpr_write_b32 a77, v33\n"
        "v_perm_b32 v34, v93, v92, s65\n"
        "v_accvgpr_write_b32 a78, v34\n"
        "v_perm_b32 v35, v95, v94, s65\n"
        "v_accvgpr_write_b32 a79, v35\n"
        "s_waitcnt vmcnt(18)\n"
        "s_barrier\n"
        "ds_read2st64_b32 v[64:65], v22 offset0:96 offset1:98\n"
        "ds_read2st64_b32 v[66:67], v22 offset0:100 offset1:102\n"
        "ds_read2st64_b32 v[68:69], v22 offset0:104 offset1:106\n"
        "ds_read2st64_b32 v[70:71], v22 offset0:108 offset1:110\n"
        "ds_read2st64_b32 v[72:73], v22 offset0:112 offset1:114\n"
        "ds_read2st64_b32 v[74:75], v22 offset0:116 offset1:118\n"
        "ds_read2st64_b32 v[76:77], v22 offset0:120 offset1:122\n"
        "ds_read2st64_b32 v[78:79], v22 offset0:124 offset1:126\n"
        "ds_read2st64_b32 v[80:81], v52 offset0:96 offset1:98\n"
        "ds_read2st64_b32 v[82:83], v52 offset0:100 offset1:102\n"
        "ds_read2st64_b32 v[84:85], v52 offset0:104 offset1:106\n"
        "ds_read2st64_b32 v[86:87], v52 offset0:108 offset1:110\n"
        "ds_read2st64_b32 v[88:89], v52 offset0:112 offset1:114\n"
        "ds_read2st64_b32 v[90:91], v52 offset0:116 offset1:118\n"
        "ds_read2st64_b32 v[92:93], v52 offset0:120 offset1:122\n"
        "ds_read2st64_b32 v[94:95], v52 offset0:124 offset1:126\n"
        "s_add_u32 s66, s70, 0\n"
        "s_add_u32 m0, s60, 16384\n"
        "s_nop 0\n"
        "buffer_load_dwordx4 v20, s[44:47], s66 offen lds\n"
        "s_mov_b32 s66, 32768\n"
        "buffer_load_dwordx2 v[208:209], v21, s[56:59], s66 offen\n"
        "s_add_u32 s66, s70, 131072\n"
        "s_add_u32 m0, s60, 17408\n"
        "s_nop 0\n"
        "buffer_load_dwordx4 v20, s[44:47], s66 offen lds\n"
        "s_mov_b32 s66, 33792\n"
        "buffer_load_dwordx2 v[210:211], v21, s[56:59], s66 offen\n"
        "buffer_store_dword v55, off, s[40:43], s73 sc1\n"
        "s_waitcnt lgkmcnt(8)\n"
        "v_perm_b32 v36, v65, v64, s64\n"
        "v_perm_b32 v37, v67, v66, s64\n"
        "v_perm_b32 v38, v69, v68, s64\n"
        "v_perm_b32 v39, v71, v70, s64\n"
        "s_add_u32 s66, s70, 16384\n"
        "s_add_u32 m0, s60, 18432\n"
        "s_nop 0\n"
        "buffer_load_dwordx4 v20, s[44:47], s66 offen lds\n"
        "s_mov_b32 s66, 40960\n"
        "buffer_load_dwordx2 v[212:213], v21, s[56:59], s66 offen\n"
        "v_perm_b32 v40, v73, v72, s64\n"
        "v_perm_b32 v41, v75, v74, s64\n"
        "v_perm_b32 v42, v77, v76, s64\n"
        "v_perm_b32 v43, v79, v78, s64\n"
        "s_add_u32 s66, s70, 147456\n"
        "s_add_u32 m0, s60, 19456\n"
        "s_nop 0\n"
        "buffer_load_dwordx4 v20, s[44:47], s66 offen lds\n"
        "s_mov_b32 s66, 41984\n"
        "buffer_load_dwordx2 v[214:215], v21, s[56:59], s66 offen\n"
        "v_perm_b32 v44, v65, v64, s65\n"
        "v_perm_b32 v45, v67, v66, s65\n"
        "v_perm_b32 v46, v69, v68, s65\n"
        "v_perm_b32 v47, v71, v70, s65\n"
        "s_add_u32 s66, s70, 524288\n"
        "s_add_u32 m0, s60, 20480\n"
        "s_nop 0\n"
        "buffer_load_dwordx4 v20, s[44:47], s66 offen lds\n"
        "s_mov_b32 s66, 34816\n"
        "buffer_load_dwordx2 v[216:217], v21, s[56:59], s66 offen\n"
        "v_perm_b32 v48, v73, v72, s65\n"
        "v_perm_b32 v49, v75, v74, s65\n"
        "v_perm_b32 v50, v77, v76, s65\n"
        "v_perm_b32 v51, v79, v78, s65\n"
        "s_add_u32 s66, s70, 655360\n"
        "s_add_u32 m0, s60, 21504\n"
        "s_nop 0\n"
        "buffer_load_dwordx4 v20, s[44:47], s66 offen lds\n"
        "s_mov_b32 s66, 35840\n"
        "buffer_load_dwordx2 v[218:219], v21, s[56:59], s66 offen\n"
        "ds_write_b128 v0, v[36:39] offset:8192\n"
        "ds_write_b128 v0, v[40:43] offset:9216\n"
        "ds_write_b128 v0, v[44:47] offset:10240\n"
        "ds_write_b128 v0, v[48:51] offset:11264\n"
        "s_waitcnt lgkmcnt(4)\n"
        "v_perm_b32 v28, v81, v80, s64\n"
        "v_accvgpr_write_b32 a80, v28\n"
        "v_perm_b32 v29, v83, v82, s64\n"
        "v_accvgpr_write_b32 a81, v29\n"
        "v_perm_b32 v30, v85, v84, s64\n"
        "v_accvgpr_write_b32 a82, v30\n"
        "v_perm_b32 v31, v87, v86, s64\n"
        "v_accvgpr_write_b32 a83, v31\n"
        "s_add_u32 s66, s70, 540672\n"
        "s_add_u32 m0, s60, 22528\n"
        "s_nop 0\n"
        "buffer_load_dwordx4 v20, s[44:47], s66 offen lds\n"
        "s_mov_b32 s66, 43008\n"
        "buffer_load_dwordx2 v[220:221], v21, s[56:59], s66 offen\n"
        "v_perm_b32 v32, v89, v88, s64\n"
        "v_accvgpr_write_b32 a84, v32\n"
        "v_perm_b32 v33, v91, v90, s64\n"
        "v_accvgpr_write_b32 a85, v33\n"
        "v_perm_b32 v34, v93, v92, s64\n"
        "v_accvgpr_write_b32 a86, v34\n"
        "v_perm_b32 v35, v95, v94, s64\n"
        "v_accvgpr_write_b32 a87, v35\n"
        "s_add_u32 s66, s70, 671744\n"
        "s_add_u32 m0, s60, 23552\n"
        "s_nop 0\n"
        "buffer_load_dwordx4 v20, s[44:47], s66 offen lds\n"
        "s_mov_b32 s66, 44032\n"
        "buffer_load_dwordx2 v[222:223], v21, s[56:59], s66 offen\n"
        "v_perm_b32 v28, v81, v80, s65\n"
        "v_accvgpr_write_b32 a88, v28\n"
        "v_perm_b32 v29, v83, v82, s65\n"
        "v_accvgpr_write_b32 a89, v29\n"
        "v_perm_b32 v30, v85, v84, s65\n"
        "v_accvgpr_write_b32 a90, v30\n"
        "v_perm_b32 v31, v87, v86, s65\n"
        "v_accvgpr_write_b32 a91, v31\n"
        "v_perm_b32 v32, v89, v88, s65\n"
        "v_accvgpr_write_b32 a92, v32\n"
        "v_perm_b32 v33, v91, v90, s65\n"
        "v_accvgpr_write_b32 a93, v33\n"
        "v_perm_b32 v34, v93, v92, s65\n"
        "v_accvgpr_write_b32 a94, v34\n"
        "v_perm_b32 v35, v95, v94, s65\n"
        "v_accvgpr_write_b32 a95, v35\n"
        "s_waitcnt vmcnt(18)\n"
        "s_barrier\n"
        "ds_read2st64_b32 v[64:65], v23 offset0:0 offset1:2\n"
        "ds_read2st64_b32 v[66:67], v23 offset0:4 offset1:6\n"
        "ds_read2st64_b32 v[68:69], v23 offset0:8 offset1:10\n"
        "ds_read2st64_b32 v[70:71], v23 offset0:12 offset1:14\n"
        "ds_read2st64_b32 v[72:73], v23 offset0:16 offset1:18\n"
        "ds_read2st64_b32 v[74:75], v23 offset0:20 offset1:22\n"
        "ds_read2st64_b32 v[76:77], v23 offset0:24 offset1:26\n"
        "ds_read2st64_b32 v[78:79], v23 offset0:28 offset1:30\n"
        "ds_read2st64_b32 v[80:81], v53 offset0:0 offset1:2\n"
        "ds_read2st64_b32 v[82:83], v53 offset0:4 offset1:6\n"
        "ds_read2st64_b32 v[84:85], v53 offset0:8 offset1:10\n"
        "ds_read2st64_b32 v[86:87], v53 offset0:12 offset1:14\n"
        "ds_read2st64_b32 v[88:89], v53 offset0:16 offset1:18\n"
        "ds_read2st64_b32 v[90:91], v53 offset0:20 offset1:22\n"
        "ds_read2st64_b32 v[92:93], v53 offset0:24 offset1:26\n"
        "ds_read2st64_b32 v[94:95], v53 offset0:28 offset1:30\n"
        "s_add_u32 s66, s70, 32768\n"
        "s_add_u32 m0, s60, 24576\n"
        "s_nop 0\n"
        "buffer_load_dwordx4 v20, s[44:47], s66 offen lds\n"
        "s_mov_b32 s66, 65536\n"
        "buffer_load_dwordx2 v[224:225], v21, s[56:59], s66 offen\n"
        "s_add_u32 s66, s70, 163840\n"
        "s_add_u32 m0, s60, 25600\n"
        "s_nop 0\n"
        "buffer_load_dwordx4 v20, s[44:47], s66 offen lds\n"
        "s_mov_b32 s66, 66560\n"
        "buffer_load_dwordx2 v[226:227], v21, s[56:59], s66 offen\n"
        "buffer_store_dword v55, off, s[40:43], s73 sc1\n"
        "s_waitcnt lgkmcnt(8)\n"
        "v_perm_b32 v36, v65, v64, s64\n"
        "v_perm_b32 v37, v67, v66, s64\n"
        "v_perm_b32 v38, v69, v68, s64\n"
        "v_perm_b32 v39, v71, v70, s64\n"
        "s_add_u32 s66, s70, 49152\n"
        "s_add_u32 m0, s60, 26624\n"
        "s_nop 0\n"
        "buffer_load_dwordx4 v20, s[44:47], s66 offen lds\n"
        "s_mov_b32 s66, 73728\n"
        "buffer_load_dwordx2 v[228:229], v21, s[56:59], s66 offen\n"
        "v_perm_b32 v40, v73, v72, s64\n"
        "v_perm_b32 v41, v75, v74, s64\n"
        "v_perm_b32 v42, v77, v76, s64\n"
        "v_perm_b32 v43, v79, v78, s64\n"
        "s_add_u32 s66, s70, 180224\n"
        "s_add_u32 m0, s60, 27648\n"
        "s_nop 0\n"
        "buffer_load_dwordx4 v20, s[44:47], s66 offen lds\n"
        "s_mov_b32 s66, 74752\n"
        "buffer_load_dwordx2 v[230:231], v21, s[56:59], s66 offen\n"
        "v_perm_b32 v44, v65, v64, s65\n"
        "v_perm_b32 v45, v67, v66, s65\n"
        "v_perm_b32 v46, v69, v68, s65\n"
        "v_perm_b32 v47, v71, v70, s65\n"
        "s_add_u32 s66, s70, 557056\n"
        "s_add_u32 m0, s60, 28672\n"
        "s_nop 0\n"
        "buffer_load_dwordx4 v20, s[44:47], s66 offen lds\n"
        "s_mov_b32 s66, 67584\n"
        "buffer_load_dwordx2 v[232:233], v21, s[56:59], s66 offen\n"
        "v_perm_b32 v48, v73, v72, s65\n"
        "v_perm_b32 v49, v75, v74, s65\n"
        "v_perm_b32 v50, v77, v76, s65\n"
        "v_perm_b32 v51, v79, v78, s65\n"
        "s_add_u32 s66, s70, 688128\n"
        "s_add_u32 m0, s60, 29696\n"
        "s_nop 0\n"
        "buffer_load_dwordx4 v20, s[44:47], s66 offen lds\n"
        "s_mov_b32 s66, 68608\n"
        "buffer_load_dwordx2 v[234:235], v21, s[56:59], s66 offen\n"
        "ds_write_b128 v0, v[36:39] offset:4096\n"
        "ds_write_b128 v0, v[40:43] offset:5120\n"
        "ds_write_b128 v0, v[44:47] offset:6144\n"
        "ds_write_b128 v0, v[48:51] offset:7168\n"
        "s_waitcnt lgkmcnt(4)\n"
        "v_perm_b32 v104, v81, v80, s64\n"
        "v_perm_b32 v105, v83, v82, s64\n"
        "v_perm_b32 v106, v85, v84, s64\n"
        "v_perm_b32 v107, v87, v86, s64\n"
        "s_add_u32 s66, s70, 573440\n"
        "s_add_u32 m0, s60, 30720\n"
        "s_nop 0\n"
        "buffer_load_dwordx4 v20, s[44:47], s66 offen lds\n"
        "s_mov_b32 s66, 75776\n"
        "buffer_load_dwordx2 v[236:237], v21, s[56:59], s66 offen\n"
        "v_perm_b32 v108, v89, v88, s64\n"
        "v_perm_b32 v109, v91, v90, s64\n"
        "v_perm_b32 v110, v93, v92, s64\n"
        "v_perm_b32 v111, v95, v94, s64\n"
        "s_add_u32 s66, s70, 704512\n"
        "s_add_u32 m0, s60, 31744\n"
        "s_nop 0\n"
        "buffer_load_dwordx4 v20, s[44:47], s66 offen lds\n"
        "s_mov_b32 s66, 76800\n"
        "buffer_load_dwordx2 v[238:239], v21, s[56:59], s66 offen\n"
        "v_perm_b32 v112, v81, v80, s65\n"
        "v_perm_b32 v113, v83, v82, s65\n"
        "v_perm_b32 v114, v85, v84, s65\n"
        "v_perm_b32 v115, v87, v86, s65\n"
        "v_perm_b32 v116, v89, v88, s65\n"
        "v_perm_b32 v117, v91, v90, s65\n"
        "v_perm_b32 v118, v93, v92, s65\n"
        "v_perm_b32 v119, v95, v94, s65\n"
        "ds_write_b128 v0, v[104:107] offset:12288\n"
        "ds_write_b128 v0, v[108:111] offset:13312\n"
        "ds_write_b128 v0, v[112:115] offset:14336\n"
        "ds_write_b128 v0, v[116:119] offset:15360\n"
        "s_waitcnt vmcnt(18)\n"
        "s_barrier\n"
        "ds_read2st64_b32 v[64:65], v22 offset0:64 offset1:66\n"
        "ds_read2st64_b32 v[66:67], v22 offset0:68 offset1:70\n"
        "ds_read2st64_b32 v[68:69], v22 offset0:72 offset1:74\n"
        "ds_read2st64_b32 v[70:71], v22 offset0:76 offset1:78\n"
        "ds_read2st64_b32 v[72:73], v22 offset0:80 offset1:82\n"
        "ds_read2st64_b32 v[74:75], v22 offset0:84 offset1:86\n"
        "ds_read2st64_b32 v[76:77], v22 offset0:88 offset1:90\n"
        "ds_read2st64_b32 v[78:79], v22 offset0:92 offset1:94\n"
        "ds_read2st64_b32 v[80:81], v52 offset0:64 offset1:66\n"
        "ds_read2st64_b32 v[82:83], v52 offset0:68 offset1:70\n"
        "ds_read2st64_b32 v[84:85], v52 offset0:72 offset1:74\n"
        "ds_read2st64_b32 v[86:87], v52 offset0:76 offset1:78\n"
        "ds_read2st64_b32 v[88:89], v52 offset0:80 offset1:82\n"
        "ds_read2st64_b32 v[90:91], v52 offset0:84 offset1:86\n"
        "ds_read2st64_b32 v[92:93], v52 offset0:88 offset1:90\n"
        "ds_read2st64_b32 v[94:95], v52 offset0:92 offset1:94\n"
        "s_add_u32 s66, s70, 0\n"
        "s_add_u32 m0, s91, 0\n"
        "s_nop 0\n"
        "buffer_load_dwordx4 v20, s[48:51], s66 offen lds\n"
        "s_mov_b32 s66, 98304\n"
        "buffer_load_dwordx2 v[240:241], v21, s[56:59], s66 offen\n"
        "s_add_u32 s66, s70, 131072\n"
        "s_add_u32 m0, s91, 1024\n"
        "s_nop 0\n"
        "buffer_load_dwordx4 v20, s[48:51], s66 offen lds\n"
        "s_mov_b32 s66, 99328\n"
        "buffer_load_dwordx2 v[242:243], v21, s[56:59], s66 offen\n"
        "s_waitcnt lgkmcnt(8)\n"
        "v_perm_b32 v28, v65, v64, s64\n"
        "v_accvgpr_write_b32 a96, v28\n"
        "v_perm_b32 v29, v67, v66, s64\n"
        "v_accvgpr_write_b32 a97, v29\n"
        "v_perm_b32 v30, v69, v68, s64\n"
        "v_accvgpr_write_b32 a98, v30\n"
        "v_perm_b32 v31, v71, v70, s64\n"
        "v_accvgpr_write_b32 a99, v31\n"
        "s_add_u32 s66, s70, 16384\n"
        "s_add_u32 m0, s91, 2048\n"
        "s_nop 0\n"
        "buffer_load_dwordx4 v20, s[48:51], s66 offen lds\n"
        "s_mov_b32 s66, 106496\n"
        "buffer_load_dwordx2 v[244:245], v21, s[56:59], s66 offen\n"
        "v_perm_b32 v32, v73, v72, s64\n"
        "v_accvgpr_write_b32 a100, v32\n"
        "v_perm_b32 v33, v75, v74, s64\n"
        "v_accvgpr_write_b32 a101, v33\n"
        "v_perm_b32 v34, v77, v76, s64\n"
        "v_accvgpr_write_b32 a102, v34\n"
        "v_perm_b32 v35, v79, v78, s64\n"
        "v_accvgpr_write_b32 a103, v35\n"
        "s_add_u32 s66, s70, 147456\n"
        "s_add_u32 m0, s91, 3072\n"
        "s_nop 0\n"
        "buffer_load_dwordx4 v20, s[48:51], s66 offen lds\n"
        "s_mov_b32 s66, 107520\n"
        "buffer_load_dwordx2 v[246:247], v21, s[56:59], s66 offen\n"
        "v_perm_b32 v28, v65, v64, s65\n"
        "v_accvgpr_write_b32 a104, v28\n"
        "v_perm_b32 v29, v67, v66, s65\n"
        "v_accvgpr_write_b32 a105, v29\n"
        "v_perm_b32 v30, v69, v68, s65\n"
        "v_accvgpr_write_b32 a106, v30\n"
        "v_perm_b32 v31, v71, v70, s65\n"
        "v_accvgpr_write_b32 a107, v31\n"
        "s_add_u32 s66, s70, 524288\n"
        "s_add_u32 m0, s91, 4096\n"
        "s_nop 0\n"
        "buffer_load_dwordx4 v20, s[48:51], s66 offen lds\n"
        "s_mov_b32 s66, 100352\n"
        "buffer_load_dwordx2 v[248:249], v21, s[56:59], s66 offen\n"
        "v_perm_b32 v32, v73, v72, s65\n"
        "v_accvgpr_write_b32 a108, v32\n"
        "v_perm_b32 v33, v75, v74, s65\n"
        "v_accvgpr_write_b32 a109, v33\n"
        "v_perm_b32 v34, v77, v76, s65\n"
        "v_accvgpr_write_b32 a110, v34\n"
        "v_perm_b32 v35, v79, v78, s65\n"
        "v_accvgpr_write_b32 a111, v35\n"
        "s_add_u32 s66, s70, 655360\n"
        "s_add_u32 m0, s91, 5120\n"
        "s_nop 0\n"
        "buffer_load_dwordx4 v20, s[48:51], s66 offen lds\n"
        "s_mov_b32 s66, 101376\n"
        "buffer_load_dwordx2 v[250:251], v21, s[56:59], s66 offen\n"
        "s_waitcnt lgkmcnt(0)\n"
        "v_perm_b32 v28, v81, v80, s64\n"
        "v_accvgpr_write_b32 a128, v28\n"
        "v_perm_b32 v29, v83, v82, s64\n"
        "v_accvgpr_write_b32 a129, v29\n"
        "v_perm_b32 v30, v85, v84, s64\n"
        "v_accvgpr_write_b32 a130, v30\n"
        "v_perm_b32 v31, v87, v86, s64\n"
        "v_accvgpr_write_b32 a131, v31\n"
        "s_add_u32 s66, s70, 540672\n"
        "s_add_u32 m0, s91, 6144\n"
        "s_nop 0\n"
        "buffer_load_dwordx4 v20, s[48:51], s66 offen lds\n"
        "s_mov_b32 s66, 108544\n"
        "buffer_load_dwordx2 v[252:253], v21, s[56:59], s66 offen\n"
        "v_perm_b32 v32, v89, v88, s64\n"
        "v_accvgpr_write_b32 a132, v32\n"
        "v_perm_b32 v33, v91, v90, s64\n"
        "v_accvgpr_write_b32 a133, v33\n"
        "v_perm_b32 v34, v93, v92, s64\n"
        "v_accvgpr_write_b32 a134, v34\n"
        "v_perm_b32 v35, v95, v94, s64\n"
        "v_accvgpr_write_b32 a135, v35\n"
        "s_add_u32 s66, s70, 671744\n"
        "s_add_u32 m0, s91, 7168\n"
        "s_nop 0\n"
        "buffer_load_dwordx4 v20, s[48:51], s66 offen lds\n"
        "s_mov_b32 s66, 109568\n"
        "buffer_load_dwordx2 v[254:255], v21, s[56:59], s66 offen\n"
        "v_perm_b32 v28, v81, v80, s65\n"
        "v_accvgpr_write_b32 a136, v28\n"
        "v_perm_b32 v29, v83, v82, s65\n"
        "v_accvgpr_write_b32 a137, v29\n"
        "v_perm_b32 v30, v85, v84, s65\n"
        "v_accvgpr_write_b32 a138, v30\n"
        "v_perm_b32 v31, v87, v86, s65\n"
        "v_accvgpr_write_b32 a139, v31\n"
        "v_perm_b32 v32, v89, v88, s65\n"
        "v_accvgpr_write_b32 a140, v32\n"
        "v_perm_b32 v33, v91, v90, s65\n"
        "v_accvgpr_write_b32 a141, v33\n"
        "v_perm_b32 v34, v93, v92, s65\n"
        "v_accvgpr_write_b32 a142, v34\n"
        "v_perm_b32 v35, v95, v94, s65\n"
        "v_accvgpr_write_b32 a143, v35\n"
        "s_waitcnt vmcnt(17)\n"
        "s_barrier\n"
        "ds_read2st64_b32 v[64:65], v22 offset0:96 offset1:98\n"
        "ds_read2st64_b32 v[66:67], v22 offset0:100 offset1:102\n"
        "ds_read2st64_b32 v[68:69], v22 offset0:104 offset1:106\n"
        "ds_read2st64_b32 v[70:71], v22 offset0:108 offset1:110\n"
        "ds_read2st64_b32 v[72:73], v22 offset0:112 offset1:114\n"
        "ds_read2st64_b32 v[74:75], v22 offset0:116 offset1:118\n"
        "ds_read2st64_b32 v[76:77], v22 offset0:120 offset1:122\n"
        "ds_read2st64_b32 v[78:79], v22 offset0:124 offset1:126\n"
        "ds_read2st64_b32 v[80:81], v52 offset0:96 offset1:98\n"
        "ds_read2st64_b32 v[82:83], v52 offset0:100 offset1:102\n"
        "ds_read2st64_b32 v[84:85], v52 offset0:104 offset1:106\n"
        "ds_read2st64_b32 v[86:87], v52 offset0:108 offset1:110\n"
        "ds_read2st64_b32 v[88:89], v52 offset0:112 offset1:114\n"
        "ds_read2st64_b32 v[90:91], v52 offset0:116 offset1:118\n"
        "ds_read2st64_b32 v[92:93], v52 offset0:120 offset1:122\n"
        "ds_read2st64_b32 v[94:95], v52 offset0:124 offset1:126\n"
        "s_add_u32 s66, s68, 32768\n"
        "s_add_u32 m0, s60, 16384\n"
        "s_nop 0\n"
        "buffer_load_dwordx4 v20, s[48:51], s66 offen lds\n"
        "s_add_u32 s66, s68, 163840\n"
        "s_add_u32 m0, s60, 17408\n"
        "s_nop 0\n"
        "buffer_load_dwordx4 v20, s[48:51], s66 offen lds\n"
        "buffer_load_dword v54, off, s[40:43], s74 sc1\n"
        "s_waitcnt lgkmcnt(8)\n"
        "v_perm_b32 v28, v65, v64, s64\n"
        "v_accvgpr_write_b32 a112, v28\n"
        "v_perm_b32 v29, v67, v66, s64\n"
        "v_accvgpr_write_b32 a113, v29\n"
        "v_perm_b32 v30, v69, v68, s64\n"
        "v_accvgpr_write_b32 a114, v30\n"
        "v_perm_b32 v31, v71, v70, s64\n"
        "v_accvgpr_write_b32 a115, v31\n"
        "s_add_u32 s66, s68, 49152\n"
        "s_add_u32 m0, s60, 18432\n"
        "s_nop 0\n"
        "buffer_load_dwordx4 v20, s[48:51], s66 offen lds\n"
        "v_perm_b32 v32, v73, v72, s64\n"
        "v_accvgpr_write_b32 a116, v32\n"
        "v_perm_b32 v33, v75, v74, s64\n"
        "v_accvgpr_write_b32 a117, v33\n"
        "v_perm_b32 v34, v77, v76, s64\n"
        "v_accvgpr_write_b32 a118, v34\n"
        "v_perm_b32 v35, v79, v78, s64\n"
        "v_accvgpr_write_b32 a119, v35\n"
        "s_add_u32 s66, s68, 180224\n"
        "s_add_u32 m0, s60, 19456\n"
        "s_nop 0\n"
        "buffer_load_dwordx4 v20, s[48:51], s66 offen lds\n"
        "v_perm_b32 v28, v65, v64, s65\n"
        "v_accvgpr_write_b32 a120, v28\n"
        "v_perm_b32 v29, v67, v66, s65\n"
        "v_accvgpr_write_b32 a121, v29\n"
        "v_perm_b32 v30, v69, v68, s65\n"
        "v_accvgpr_write_b32 a122, v30\n"
        "v_perm_b32 v31, v71, v70, s65\n"
        "v_accvgpr_write_b32 a123, v31\n"
        "s_add_u32 s66, s68, 557056\n"
        "s_add_u32 m0, s60, 20480\n"
        "s_nop 0\n"
        "buffer_load_dwordx4 v20, s[48:51], s66 offen lds\n"
        "v_perm_b32 v32, v73, v72, s65\n"
        "v_accvgpr_write_b32 a124, v32\n"
        "v_perm_b32 v33, v75, v74, s65\n"
        "v_accvgpr_write_b32 a125, v33\n"
        "v_perm_b32 v34, v77, v76, s65\n"
        "v_accvgpr_write_b32 a126, v34\n"
        "v_perm_b32 v35, v79, v78, s65\n"
        "v_accvgpr_write_b32 a127, v35\n"
        "s_add_u32 s66, s68, 688128\n"
        "s_add_u32 m0, s60, 21504\n"
        "s_nop 0\n"
        "buffer_load_dwordx4 v20, s[48:51], s66 offen lds\n"
        "s_waitcnt lgkmcnt(0)\n"
        "v_perm_b32 v28, v81, v80, s64\n"
        "v_accvgpr_write_b32 a144, v28\n"
        "v_perm_b32 v29, v83, v82, s64\n"
        "v_accvgpr_write_b32 a145, v29\n"
        "v_perm_b32 v30, v85, v84, s64\n"
        "v_accvgpr_write_b32 a146, v30\n"
        "v_perm_b32 v31, v87, v86, s64\n"
        "v_accvgpr_write_b32 a147, v31\n"
        "s_add_u32 s66, s68, 573440\n"
        "s_add_u32 m0, s60, 22528\n"
        "s_nop 0\n"
        "buffer_load_dwordx4 v20, s[48:51], s66 offen lds\n"
        "v_perm_b32 v32, v89, v88, s64\n"
        "v_accvgpr_write_b32 a148, v32\n"
        "v_perm_b32 v33, v91, v90, s64\n"
        "v_accvgpr_write_b32 a149, v33\n"
        "v_perm_b32 v34, v93, v92, s64\n"
        "v_accvgpr_write_b32 a150, v34\n"
        "v_perm_b32 v35, v95, v94, s64\n"
        "v_accvgpr_write_b32 a151, v35\n"
        "s_add_u32 s66, s68, 704512\n"
        "s_add_u32 m0, s60, 23552\n"
        "s_nop 0\n"
        "buffer_load_dwordx4 v20, s[48:51], s66 offen lds\n"
        "v_perm_b32 v28, v81, v80, s65\n"
        "v_accvgpr_write_b32 a152, v28\n"
        "v_perm_b32 v29, v83, v82, s65\n"
        "v_accvgpr_write_b32 a153, v29\n"
        "v_perm_b32 v30, v85, v84, s65\n"
        "v_accvgpr_write_b32 a154, v30\n"
        "v_perm_b32 v31, v87, v86, s65\n"
        "v_accvgpr_write_b32 a155, v31\n"
        "v_perm_b32 v32, v89, v88, s65\n"
        "v_accvgpr_write_b32 a156, v32\n"
        "v_perm_b32 v33, v91, v90, s65\n"
        "v_accvgpr_write_b32 a157, v33\n"
        "v_perm_b32 v34, v93, v92, s65\n"
        "v_accvgpr_write_b32 a158, v34\n"
        "v_perm_b32 v35, v95, v94, s65\n"
        "v_accvgpr_write_b32 a159, v35\n"
        "v_perm_b32 v28, v194, v192, s64\n"
        "v_accvgpr_write_b32 a0, v28\n"
        "v_perm_b32 v29, v198, v196, s64\n"
        "v_accvgpr_write_b32 a1, v29\n"
        "v_perm_b32 v30, v202, v200, s64\n"
        "v_accvgpr_write_b32 a2, v30\n"
        "v_perm_b32 v31, v206, v204, s64\n"
        "v_accvgpr_write_b32 a3, v31\n"
        "v_perm_b32 v32, v210, v208, s64\n"
        "v_accvgpr_write_b32 a4, v32\n"
        "v_perm_b32 v33, v214, v212, s64\n"
        "v_accvgpr_write_b32 a5, v33\n"
        "v_perm_b32 v34, v218, v216, s64\n"
        "v_accvgpr_write_b32 a6, v34\n"
        "v_perm_b32 v35, v222, v220, s64\n"
        "v_accvgpr_write_b32 a7, v35\n"
        "v_perm_b32 v28, v194, v192, s65\n"
        "v_accvgpr_write_b32 a16, v28\n"
        "v_perm_b32 v29, v198, v196, s65\n"
        "v_accvgpr_write_b32 a17, v29\n"
        "v_perm_b32 v30, v202, v200, s65\n"
        "v_accvgpr_write_b32 a18, v30\n"
        "v_perm_b32 v31, v206, v204, s65\n"
        "v_accvgpr_write_b32 a19, v31\n"
        "v_perm_b32 v32, v210, v208, s65\n"
        "v_accvgpr_write_b32 a20, v32\n"
        "v_perm_b32 v33, v214, v212, s65\n"
        "v_accvgpr_write_b32 a21, v33\n"
        "v_perm_b32 v34, v218, v216, s65\n"
        "v_accvgpr_write_b32 a22, v34\n"
        "v_perm_b32 v35, v222, v220, s65\n"
        "v_accvgpr_write_b32 a23, v35\n"
        "v_perm_b32 v28, v195, v193, s64\n"
        "v_accvgpr_write_b32 a32, v28\n"
        "v_perm_b32 v29, v199, v197, s64\n"
        "v_accvgpr_write_b32 a33, v29\n"
        "v_perm_b32 v30, v203, v201, s64\n"
        "v_accvgpr_write_b32 a34, v30\n"
        "v_perm_b32 v31, v207, v205, s64\n"
        "v_accvgpr_write_b32 a35, v31\n"
        "v_perm_b32 v32, v211, v209, s64\n"
        "v_accvgpr_write_b32 a36, v32\n"
        "v_perm_b32 v33, v215, v213, s64\n"
        "v_accvgpr_write_b32 a37, v33\n"
        "v_perm_b32 v34, v219, v217, s64\n"
        "v_accvgpr_write_b32 a38, v34\n"
        "v_perm_b32 v35, v223, v221, s64\n"
        "v_accvgpr_write_b32 a39, v35\n"
        "v_perm_b32 v28, v195, v193, s65\n"
        "v_accvgpr_write_b32 a48, v28\n"
        "v_perm_b32 v29, v199, v197, s65\n"
        "v_accvgpr_write_b32 a49, v29\n"
        "v_perm_b32 v30, v203, v201, s65\n"
        "v_accvgpr_write_b32 a50, v30\n"
        "v_perm_b32 v31, v207, v205, s65\n"
        "v_accvgpr_write_b32 a51, v31\n"
        "v_perm_b32 v32, v211, v209, s65\n"
        "v_accvgpr_write_b32 a52, v32\n"
        "v_perm_b32 v33, v215, v213, s65\n"
        "v_accvgpr_write_b32 a53, v33\n"
        "v_perm_b32 v34, v219, v217, s65\n"
        "v_accvgpr_write_b32 a54, v34\n"
        "v_perm_b32 v35, v223, v221, s65\n"
        "v_accvgpr_write_b32 a55, v35\n"
        "s_waitcnt vmcnt(10)\n"
        "s_barrier\n"
        "ds_read2st64_b32 v[64:65], v23 offset0:0 offset1:2\n"
        "ds_read2st64_b32 v[66:67], v23 offset0:4 offset1:6\n"
        "ds_read2st64_b32 v[68:69], v23 offset0:8 offset1:10\n"
        "ds_read2st64_b32 v[70:71], v23 offset0:12 offset1:14\n"
        "ds_read2st64_b32 v[72:73], v23 offset0:16 offset1:18\n"
        "ds_read2st64_b32 v[74:75], v23 offset0:20 offset1:22\n"
        "ds_read2st64_b32 v[76:77], v23 offset0:24 offset1:26\n"
        "ds_read2st64_b32 v[78:79], v23 offset0:28 offset1:30\n"
        "ds_read2st64_b32 v[80:81], v53 offset0:0 offset1:2\n"
        "ds_read2st64_b32 v[82:83], v53 offset0:4 offset1:6\n"
        "ds_read2st64_b32 v[84:85], v53 offset0:8 offset1:10\n"
        "ds_read2st64_b32 v[86:87], v53 offset0:12 offset1:14\n"
        "ds_read2st64_b32 v[88:89], v53 offset0:16 offset1:18\n"
        "ds_read2st64_b32 v[90:91], v53 offset0:20 offset1:22\n"
        "ds_read2st64_b32 v[92:93], v53 offset0:24 offset1:26\n"
        "ds_read2st64_b32 v[94:95], v53 offset0:28 offset1:30\n"
        "s_add_u32 s66, s68, 0\n"
        "s_add_u32 m0, s60, 24576\n"
        "s_nop 0\n"
        "buffer_load_dwordx4 v20, s[52:55], s66 offen lds\n"
        "s_add_u32 s66, s68, 131072\n"
        "s_add_u32 m0, s60, 25600\n"
        "s_nop 0\n"
        "buffer_load_dwordx4 v20, s[52:55], s66 offen lds\n"
        "s_waitcnt lgkmcnt(8)\n"
        "v_perm_b32 v28, v65, v64, s64\n"
        "v_accvgpr_write_b32 a192, v28\n"
        "v_perm_b32 v29, v67, v66, s64\n"
        "v_accvgpr_write_b32 a193, v29\n"
        "v_perm_b32 v30, v69, v68, s64\n"
        "v_accvgpr_write_b32 a194, v30\n"
        "v_perm_b32 v31, v71, v70, s64\n"
        "v_accvgpr_write_b32 a195, v31\n"
        "s_add_u32 s66, s68, 16384\n"
        "s_add_u32 m0, s60, 26624\n"
        "s_nop 0\n"
        "buffer_load_dwordx4 v20, s[52:55], s66 offen lds\n"
        "v_perm_b32 v32, v73, v72, s64\n"
        "v_accvgpr_write_b32 a196, v32\n"
        "v_perm_b32 v33, v75, v74, s64\n"
        "v_accvgpr_write_b32 a197, v33\n"
        "v_perm_b32 v34, v77, v76, s64\n"
        "v_accvgpr_write_b32 a198, v34\n"
        "v_perm_b32 v35, v79, v78, s64\n"
        "v_accvgpr_write_b32 a199, v35\n"
        "s_add_u32 s66, s68, 147456\n"
        "s_add_u32 m0, s60, 27648\n"
        "s_nop 0\n"
        "buffer_load_dwordx4 v20, s[52:55], s66 offen lds\n"
        "v_perm_b32 v28, v65, v64, s65\n"
        "v_accvgpr_write_b32 a200, v28\n"
        "v_perm_b32 v29, v67, v66, s65\n"
        "v_accvgpr_write_b32 a201, v29\n"
        "v_perm_b32 v30, v69, v68, s65\n"
        "v_accvgpr_write_b32 a202, v30\n"
        "v_perm_b32 v31, v71, v70, s65\n"
        "v_accvgpr_write_b32 a203, v31\n"
        "s_add_u32 s66, s68, 524288\n"
        "s_add_u32 m0, s60, 28672\n"
        "s_nop 0\n"
        "buffer_load_dwordx4 v20, s[52:55], s66 offen lds\n"
        "v_perm_b32 v32, v73, v72, s65\n"
        "v_accvgpr_write_b32 a204, v32\n"
        "v_perm_b32 v33, v75, v74, s65\n"
        "v_accvgpr_write_b32 a205, v33\n"
        "v_perm_b32 v34, v77, v76, s65\n"
        "v_accvgpr_write_b32 a206, v34\n"
        "v_perm_b32 v35, v79, v78, s65\n"
        "v_accvgpr_write_b32 a207, v35\n"
        "s_add_u32 s66, s68, 655360\n"
        "s_add_u32 m0, s60, 29696\n"
        "s_nop 0\n"
        "buffer_load_dwordx4 v20, s[52:55], s66 offen lds\n"
        "s_waitcnt lgkmcnt(0)\n"
        "v_perm_b32 v28, v81, v80, s64\n"
        "v_accvgpr_write_b32 a224, v28\n"
        "v_perm_b32 v29, v83, v82, s64\n"
        "v_accvgpr_write_b32 a225, v29\n"
        "v_perm_b32 v30, v85, v84, s64\n"
        "v_accvgpr_write_b32 a226, v30\n"
        "v_perm_b32 v31, v87, v86, s64\n"
        "v_accvgpr_write_b32 a227, v31\n"
        "s_add_u32 s66, s68, 540672\n"
        "s_add_u32 m0, s60, 30720\n"
        "s_nop 0\n"
        "buffer_load_dwordx4 v20, s[52:55], s66 offen lds\n"
        "v_perm_b32 v32, v89, v88, s64\n"
        "v_accvgpr_write_b32 a228, v32\n"
        "v_perm_b32 v33, v91, v90, s64\n"
        "v_accvgpr_write_b32 a229, v33\n"
        "v_perm_b32 v34, v93, v92, s64\n"
        "v_accvgpr_write_b32 a230, v34\n"
        "v_perm_b32 v35, v95, v94, s64\n"
        "v_accvgpr_write_b32 a231, v35\n"
        "s_add_u32 s66, s68, 671744\n"
        "s_add_u32 m0, s60, 31744\n"
        "s_nop 0\n"
        "buffer_load_dwordx4 v20, s[52:55], s66 offen lds\n"
        "v_perm_b32 v28, v81, v80, s65\n"
        "v_accvgpr_write_b32 a232, v28\n"
        "v_perm_b32 v29, v83, v82, s65\n"
        "v_accvgpr_write_b32 a233, v29\n"
        "v_perm_b32 v30, v85, v84, s65\n"
        "v_accvgpr_write_b32 a234, v30\n"
        "v_perm_b32 v31, v87, v86, s65\n"
        "v_accvgpr_write_b32 a235, v31\n"
        "v_perm_b32 v32, v89, v88, s65\n"
        "v_accvgpr_write_b32 a236, v32\n"
        "v_perm_b32 v33, v91, v90, s65\n"
        "v_accvgpr_write_b32 a237, v33\n"
        "v_perm_b32 v34, v93, v92, s65\n"
        "v_accvgpr_write_b32 a238, v34\n"
        "v_perm_b32 v35, v95, v94, s65\n"
        "v_accvgpr_write_b32 a239, v35\n"
        "s_waitcnt vmcnt(8)\n"
        "s_barrier\n"
        "ds_read2st64_b32 v[64:65], v22 offset0:64 offset1:66\n"
        "ds_read2st64_b32 v[66:67], v22 offset0:68 offset1:70\n"
        "ds_read2st64_b32 v[68:69], v22 offset0:72 offset1:74\n"
        "ds_read2st64_b32 v[70:71], v22 offset0:76 offset1:78\n"
        "ds_read2st64_b32 v[72:73], v22 offset0:80 offset1:82\n"
        "ds_read2st64_b32 v[74:75], v22 offset0:84 offset1:86\n"
        "ds_read2st64_b32 v[76:77], v22 offset0:88 offset1:90\n"
        "ds_read2st64_b32 v[78:79], v22 offset0:92 offset1:94\n"
        "ds_read2st64_b32 v[80:81], v52 offset0:64 offset1:66\n"
        "ds_read2st64_b32 v[82:83], v52 offset0:68 offset1:70\n"
        "ds_read2st64_b32 v[84:85], v52 offset0:72 offset1:74\n"
        "ds_read2st64_b32 v[86:87], v52 offset0:76 offset1:78\n"
        "ds_read2st64_b32 v[88:89], v52 offset0:80 offset1:82\n"
        "ds_read2st64_b32 v[90:91], v52 offset0:84 offset1:86\n"
        "ds_read2st64_b32 v[92:93], v52 offset0:88 offset1:90\n"
        "ds_read2st64_b32 v[94:95], v52 offset0:92 offset1:94\n"
        "s_add_u32 s66, s68, 32768\n"
        "s_add_u32 m0, s91, 0\n"
        "s_nop 0\n"
        "buffer_load_dwordx4 v20, s[52:55], s66 offen lds\n"
        "s_add_u32 s66, s68, 163840\n"
        "s_add_u32 m0, s91, 1024\n"
        "s_nop 0\n"
        "buffer_load_dwordx4 v20, s[52:55], s66 offen lds\n"
        "s_waitcnt lgkmcnt(8)\n"
        "v_perm_b32 v28, v65, v64, s64\n"
        "v_accvgpr_write_b32 a160, v28\n"
        "v_perm_b32 v29, v67, v66, s64\n"
        "v_accvgpr_write_b32 a161, v29\n"
        "v_perm_b32 v30, v69, v68, s64\n"
        "v_accvgpr_write_b32 a162, v30\n"
        "v_perm_b32 v31, v71, v70, s64\n"
        "v_accvgpr_write_b32 a163, v31\n"
        "s_add_u32 s66, s68, 49152\n"
        "s_add_u32 m0, s91, 2048\n"
        "s_nop 0\n"
        "buffer_load_dwordx4 v20, s[52:55], s66 offen lds\n"
        "v_perm_b32 v32, v73, v72, s64\n"
        "v_accvgpr_write_b32 a164, v32\n"
        "v_perm_b32 v33, v75, v74, s64\n"
        "v_accvgpr_write_b32 a165, v33\n"
        "v_perm_b32 v34, v77, v76, s64\n"
        "v_accvgpr_write_b32 a166, v34\n"
        "v_perm_b32 v35, v79, v78, s64\n"
        "v_accvgpr_write_b32 a167, v35\n"
        "s_add_u32 s66, s68, 180224\n"
        "s_add_u32 m0, s91, 3072\n"
        "s_nop 0\n"
        "buffer_load_dwordx4 v20, s[52:55], s66 offen lds\n"
        "v_perm_b32 v28, v65, v64, s65\n"
        "v_accvgpr_write_b32 a168, v28\n"
        "v_perm_b32 v29, v67, v66, s65\n"
        "v_accvgpr_write_b32 a169, v29\n"
        "v_perm_b32 v30, v69, v68, s65\n"
        "v_accvgpr_write_b32 a170, v30\n"
        "v_perm_b32 v31, v71, v70, s65\n"
        "v_accvgpr_write_b32 a171, v31\n"
        "s_add_u32 s66, s68, 557056\n"
        "s_add_u32 m0, s91, 4096\n"
        "s_nop 0\n"
        "buffer_load_dwordx4 v20, s[52:55], s66 offen lds\n"
        "v_perm_b32 v32, v73, v72, s65\n"
        "v_accvgpr_write_b32 a172, v32\n"
        "v_perm_b32 v33, v75, v74, s65\n"
        "v_accvgpr_write_b32 a173, v33\n"
        "v_perm_b32 v34, v77, v76, s65\n"
        "v_accvgpr_write_b32 a174, v34\n"
        "v_perm_b32 v35, v79, v78, s65\n"
        "v_accvgpr_write_b32 a175, v35\n"
        "s_add_u32 s66, s68, 688128\n"
        "s_add_u32 m0, s91, 5120\n"
        "s_nop 0\n"
        "buffer_load_dwordx4 v20, s[52:55], s66 offen lds\n"
        "s_waitcnt lgkmcnt(0)\n"
        "v_perm_b32 v28, v81, v80, s64\n"
        "v_accvgpr_write_b32 a176, v28\n"
        "v_perm_b32 v29, v83, v82, s64\n"
        "v_accvgpr_write_b32 a177, v29\n"
        "v_perm_b32 v30, v85, v84, s64\n"
        "v_accvgpr_write_b32 a178, v30\n"
        "v_perm_b32 v31, v87, v86, s64\n"
        "v_accvgpr_write_b32 a179, v31\n"
        "s_add_u32 s66, s68, 573440\n"
        "s_add_u32 m0, s91, 6144\n"
        "s_nop 0\n"
        "buffer_load_dwordx4 v20, s[52:55], s66 offen lds\n"
        "v_perm_b32 v32, v89, v88, s64\n"
        "v_accvgpr_write_b32 a180, v32\n"
        "v_perm_b32 v33, v91, v90, s64\n"
        "v_accvgpr_write_b32 a181, v33\n"
        "v_perm_b32 v34, v93, v92, s64\n"
        "v_accvgpr_write_b32 a182, v34\n"
        "v_perm_b32 v35, v95, v94, s64\n"
        "v_accvgpr_write_b32 a183, v35\n"
        "s_add_u32 s66, s68, 704512\n"
        "s_add_u32 m0, s91, 7168\n"
        "s_nop 0\n"
        "buffer_load_dwordx4 v20, s[52:55], s66 offen lds\n"
        "v_perm_b32 v28, v81, v80, s65\n"
        "v_accvgpr_write_b32 a184, v28\n"
        "v_perm_b32 v29, v83, v82, s65\n"
        "v_accvgpr_write_b32 a185, v29\n"
        "v_perm_b32 v30, v85, v84, s65\n"
        "v_accvgpr_write_b32 a186, v30\n"
        "v_perm_b32 v31, v87, v86, s65\n"
        "v_accvgpr_write_b32 a187, v31\n"
        "v_perm_b32 v32, v89, v88, s65\n"
        "v_accvgpr_write_b32 a188, v32\n"
        "v_perm_b32 v33, v91, v90, s65\n"
        "v_accvgpr_write_b32 a189, v33\n"
        "v_perm_b32 v34, v93, v92, s65\n"
        "v_accvgpr_write_b32 a190, v34\n"
        "v_perm_b32 v35, v95, v94, s65\n"
        "v_accvgpr_write_b32 a191, v35\n"
        "v_perm_b32 v28, v226, v224, s64\n"
        "v_accvgpr_write_b32 a8, v28\n"
        "v_perm_b32 v29, v230, v228, s64\n"
        "v_accvgpr_write_b32 a9, v29\n"
        "v_perm_b32 v30, v234, v232, s64\n"
        "v_accvgpr_write_b32 a10, v30\n"
        "v_perm_b32 v31, v238, v236, s64\n"
        "v_accvgpr_write_b32 a11, v31\n"
        "v_perm_b32 v32, v242, v240, s64\n"
        "v_accvgpr_write_b32 a12, v32\n"
        "v_perm_b32 v33, v246, v244, s64\n"
        "v_accvgpr_write_b32 a13, v33\n"
        "v_perm_b32 v34, v250, v248, s64\n"
        "v_accvgpr_write_b32 a14, v34\n"
        "v_perm_b32 v35, v254, v252, s64\n"
        "v_accvgpr_write_b32 a15, v35\n"
        "v_perm_b32 v28, v226, v224, s65\n"
        "v_accvgpr_write_b32 a24, v28\n"
        "v_perm_b32 v29, v230, v228, s65\n"
        "v_accvgpr_write_b32 a25, v29\n"
        "v_perm_b32 v30, v234, v232, s65\n"
        "v_accvgpr_write_b32 a26, v30\n"
        "v_perm_b32 v31, v238, v236, s65\n"
        "v_accvgpr_write_b32 a27, v31\n"
        "v_perm_b32 v32, v242, v240, s65\n"
        "v_accvgpr_write_b32 a28, v32\n"
        "v_perm_b32 v33, v246, v244, s65\n"
        "v_accvgpr_write_b32 a29, v33\n"
        "v_perm_b32 v34, v250, v248, s65\n"
        "v_accvgpr_write_b32 a30, v34\n"
        "v_perm_b32 v35, v254, v252, s65\n"
        "v_accvgpr_write_b32 a31, v35\n"
        "v_perm_b32 v28, v227, v225, s64\n"
        "v_accvgpr_write_b32 a40, v28\n"
        "v_perm_b32 v29, v231, v229, s64\n"
        "v_accvgpr_write_b32 a41, v29\n"
        "v_perm_b32 v30, v235, v233, s64\n"
        "v_accvgpr_write_b32 a42, v30\n"
        "v_perm_b32 v31, v239, v237, s64\n"
        "v_accvgpr_write_b32 a43, v31\n"
        "v_perm_b32 v32, v243, v241, s64\n"
        "v_accvgpr_write_b32 a44, v32\n"
        "v_perm_b32 v33, v247, v245, s64\n"
        "v_accvgpr_write_b32 a45, v33\n"
        "v_perm_b32 v34, v251, v249, s64\n"
        "v_accvgpr_write_b32 a46, v34\n"
        "v_perm_b32 v35, v255, v253, s64\n"
        "v_accvgpr_write_b32 a47, v35\n"
        "v_perm_b32 v28, v227, v225, s65\n"
        "v_accvgpr_write_b32 a56, v28\n"
        "v_perm_b32 v29, v231, v229, s65\n"
        "v_accvgpr_write_b32 a57, v29\n"
        "v_perm_b32 v30, v235, v233, s65\n"
        "v_accvgpr_write_b32 a58, v30\n"
        "v_perm_b32 v31, v239, v237, s65\n"
        "v_accvgpr_write_b32 a59, v31\n"
        "v_perm_b32 v32, v243, v241, s65\n"
        "v_accvgpr_write_b32 a60, v32\n"
        "v_perm_b32 v33, v247, v245, s65\n"
        "v_accvgpr_write_b32 a61, v33\n"
        "v_perm_b32 v34, v251, v249, s65\n"
        "v_accvgpr_write_b32 a62, v34\n"
        "v_perm_b32 v35, v255, v253, s65\n"
        "v_accvgpr_write_b32 a63, v35\n"
        "s_waitcnt vmcnt(8)\n"
        "s_barrier\n"
        "ds_read2st64_b32 v[64:65], v22 offset0:96 offset1:98\n"
        "ds_read2st64_b32 v[66:67], v22 offset0:100 offset1:102\n"
        "ds_read2st64_b32 v[68:69], v22 offset0:104 offset1:106\n"
        "ds_read2st64_b32 v[70:71], v22 offset0:108 offset1:110\n"
        "ds_read2st64_b32 v[72:73], v22 offset0:112 offset1:114\n"
        "ds_read2st64_b32 v[74:75], v22 offset0:116 offset1:118\n"
        "ds_read2st64_b32 v[76:77], v22 offset0:120 offset1:122\n"
        "ds_read2st64_b32 v[78:79], v22 offset0:124 offset1:126\n"
        "ds_read2st64_b32 v[80:81], v52 offset0:96 offset1:98\n"
        "ds_read2st64_b32 v[82:83], v52 offset0:100 offset1:102\n"
        "ds_read2st64_b32 v[84:85], v52 offset0:104 offset1:106\n"
        "ds_read2st64_b32 v[86:87], v52 offset0:108 offset1:110\n"
        "ds_read2st64_b32 v[88:89], v52 offset0:112 offset1:114\n"
        "ds_read2st64_b32 v[90:91], v52 offset0:116 offset1:118\n"
        "ds_read2st64_b32 v[92:93], v52 offset0:120 offset1:122\n"
        "ds_read2st64_b32 v[94:95], v52 offset0:124 offset1:126\n"
        "s_add_u32 s66, s70, 32768\n"
        "s_add_u32 m0, s60, 16384\n"
        "s_nop 0\n"
        "buffer_load_dwordx4 v20, s[48:51], s66 offen lds\n"
        "s_add_u32 s66, s70, 163840\n"
        "s_add_u32 m0, s60, 17408\n"
        "s_nop 0\n"
        "buffer_load_dwordx4 v20, s[48:51], s66 offen lds\n"
        "s_mov_b32 s81, 0\n"
        "s_cmp_gt_u32 s63, 255\n"
        "s_cbranch_scc1 LR_h2\n"
        "s_branch LR_h1\n"
        "LR_h0:\n"
        "buffer_store_dword v55, off, s[40:43], s73 sc1\n"
        "s_sleep 4\n"
        "buffer_load_dword v54, off, s[40:43], s74 sc1\n"
        "s_add_u32 s81, s81, 1\n"
        "s_cmp_gt_u32 s81, 0x40000\n"
        "s_cbranch_scc1 LR_end\n"
        "s_waitcnt vmcnt(0)\n"
        "LR_h1:\n"
        "v_readfirstlane_b32 s66, v54\n"
        "s_nop 1\n"
        "s_or_b32 s67, s66, 8\n"
        "s_cmp_lg_u32 s67, s72\n"
        "s_cbranch_scc1 LR_h0\n"
        "s_bitcmp1_b32 s66, 3\n"
        "s_cbranch_scc0 LR_restart\n"
        "LR_h2:\n"
        "v_lshlrev_b32 v125, 3, v9\n"
        "v_and_b32 v125, 3968, v125\n"
        "s_mov_b32 s81, 0\n"
        "s_mov_b32 s66, 4222976\n"
        "buffer_load_dword v124, v125, s[40:43], s66 offen sc1\n"
        "s_waitcnt lgkmcnt(8)\n"
        "v_perm_b32 v128, v65, v64, s64\n"
        "v_perm_b32 v129, v67, v66, s64\n"
        "v_perm_b32 v130, v69, v68, s64\n"
        "v_perm_b32 v131, v71, v70, s64\n"
        "s_add_u32 s66, s70, 49152\n"
        "s_add_u32 m0, s60, 18432\n"
        "s_nop 0\n"
        "buffer_load_dwordx4 v20, s[48:51], s66 offen lds\n"
        "v_perm_b32 v132, v73, v72, s64\n"
        "v_perm_b32 v133, v75, v74, s64\n"
        "v_perm_b32 v134, v77, v76, s64\n"
        "v_perm_b32 v135, v79, v78, s64\n"
        "s_add_u32 s66, s70, 180224\n"
        "s_add_u32 m0, s60, 19456\n"
        "s_nop 0\n"
        "buffer_load_dwordx4 v20, s[48:51], s66 offen lds\n"
        "v_perm_b32 v136, v65, v64, s65\n"
        "v_perm_b32 v137, v67, v66, s65\n"
        "v_perm_b32 v138, v69, v68, s65\n"
        "v_perm_b32 v139, v71, v70, s65\n"
        "s_add_u32 s66, s70, 557056\n"
        "s_add_u32 m0, s60, 20480\n"
        "s_nop 0\n"
        "buffer_load_dwordx4 v20, s[48:51], s66 offen lds\n"
        "v_perm_b32 v140, v73, v72, s65\n"
        "v_perm_b32 v141, v75, v74, s65\n"
        "v_perm_b32 v142, v77, v76, s65\n"
        "v_perm_b32 v143, v79, v78, s65\n"
        "s_add_u32 s66, s70, 688128\n"
        "s_add_u32 m0, s60, 21504\n"
        "s_nop 0\n"
        "buffer_load_dwordx4 v20, s[48:51], s66 offen lds\n"
        "s_waitcnt lgkmcnt(0)\n"
        "v_perm_b32 v160, v81, v80, s64\n"
        "v_perm_b32 v161, v83, v82, s64\n"
        "v_perm_b32 v162, v85, v84, s64\n"
        "v_perm_b32 v163, v87, v86, s64\n"
        "s_add_u32 s66, s70, 573440\n"
        "s_add_u32 m0, s60, 22528\n"
        "s_nop 0\n"
        "buffer_load_dwordx4 v20, s[48:51], s66 offen lds\n"
        "v_perm_b32 v164, v89, v88, s64\n"
        "v_perm_b32 v165, v91, v90, s64\n"
        "v_perm_b32 v166, v93, v92, s64\n"
        "v_perm_b32 v167, v95, v94, s64\n"
        "s_add_u32 s66, s70, 704512\n"
        "s_add_u32 m0, s60, 23552\n"
        "s_nop 0\n"
        "buffer_load_dwordx4 v20, s[48:51], s66 offen lds\n"
        "v_perm_b32 v168, v81, v80, s65\n"
        "v_perm_b32 v169, v83, v82, s65\n"
        "v_perm_b32 v170, v85, v84, s65\n"
        "v_perm_b32 v171, v87, v86, s65\n"
        "v_perm_b32 v172, v89, v88, s65\n"
        "v_perm_b32 v173, v91, v90, s65\n"
        "v_perm_b32 v174, v93, v92, s65\n"
        "v_perm_b32 v175, v95, v94, s65\n"
        "s_waitcnt vmcnt(9)\n"
        "s_barrier\n"
        "ds_read2st64_b32 v[64:65], v23 offset0:0 offset1:2\n"
        "ds_read2st64_b32 v[66:67], v23 offset0:4 offset1:6\n"
        "ds_read2st64_b32 v[68:69], v23 offset0:8 offset1:10\n"
        "ds_read2st64_b32 v[70:71], v23 offset0:12 offset1:14\n"
        "ds_read2st64_b32 v[72:73], v23 offset0:16 offset1:18\n"
        "ds_read2st64_b32 v[74:75], v23 offset0:20 offset1:22\n"
        "ds_read2st64_b32 v[76:77], v23 offset0:24 offset1:26\n"
        "ds_read2st64_b32 v[78:79], v23 offset0:28 offset1:30\n"
        "ds_read2st64_b32 v[80:81], v53 offset0:0 offset1:2\n"
        "ds_read2st64_b32 v[82:83], v53 offset0:4 offset1:6\n"
        "ds_read2st64_b32 v[84:85], v53 offset0:8 offset1:10\n"
        "ds_read2st64_b32 v[86:87], v53 offset0:12 offset1:14\n"
        "ds_read2st64_b32 v[88:89], v53 offset0:16 offset1:18\n"
        "ds_read2st64_b32 v[90:91], v53 offset0:20 offset1:22\n"
        "ds_read2st64_b32 v[92:93], v53 offset0:24 offset1:26\n"
        "ds_read2st64_b32 v[94:95], v53 offset0:28 offset1:30\n"
        "s_add_u32 s66, s70, 0\n"
        "s_add_u32 m0, s60, 24576\n"
        "s_nop 0\n"
        "buffer_load_dwordx4 v20, s[52:55], s66 offen lds\n"
        "s_add_u32 s66, s70, 131072\n"
        "s_add_u32 m0, s60, 25600\n"
        "s_nop 0\n"
        "buffer_load_dwordx4 v20, s[52:55], s66 offen lds\n"
        "s_waitcnt lgkmcnt(8)\n"
        "v_perm_b32 v144, v65, v64, s64\n"
        "v_perm_b32 v145, v67, v66, s64\n"
        "v_perm_b32 v146, v69, v68, s64\n"
        "v_perm_b32 v147, v71, v70, s64\n"
        "s_add_u32 s66, s70, 16384\n"
        "s_add_u32 m0, s60, 26624\n"
        "s_nop 0\n"
        "buffer_load_dwordx4 v20, s[52:55], s66 offen lds\n"
        "v_perm_b32 v148, v73, v72, s64\n"
        "v_perm_b32 v149, v75, v74, s64\n"
        "v_perm_b32 v150, v77, v76, s64\n"
        "v_perm_b32 v151, v79, v78, s64\n"
        "s_add_u32 s66, s70, 147456\n"
        "s_add_u32 m0, s60, 27648\n"
        "s_nop 0\n"
        "buffer_load_dwordx4 v20, s[52:55], s66 offen lds\n"
        "v_perm_b32 v152, v65, v64, s65\n"
        "v_perm_b32 v153, v67, v66, s65\n"
        "v_perm_b32 v154, v69, v68, s65\n"
        "v_perm_b32 v155, v71, v70, s65\n"
        "s_add_u32 s66, s70, 524288\n"
        "s_add_u32 m0, s60, 28672\n"
        "s_nop 0\n"
        "buffer_load_dwordx4 v20, s[52:55], s66 offen lds\n"
        "v_perm_b32 v156, v73, v72, s65\n"
        "v_perm_b32 v157, v75, v74, s65\n"
        "v_perm_b32 v158, v77, v76, s65\n"
        "v_perm_b32 v159, v79, v78, s65\n"
        "s_add_u32 s66, s70, 655360\n"
        "s_add_u32 m0, s60, 29696\n"
        "s_nop 0\n"
        "buffer_load_dwordx4 v20, s[52:55], s66 offen lds\n"
        "s_waitcnt lgkmcnt(0)\n"
        "v_perm_b32 v176, v81, v80, s64\n"
        "v_perm_b32 v177, v83, v82, s64\n"
        "v_perm_b32 v178, v85, v84, s64\n"
        "v_perm_b32 v179, v87, v86, s64\n"
        "s_add_u32 s66, s70, 540672\n"
        "s_add_u32 m0, s60, 30720\n"
        "s_nop 0\n"
        "buffer_load_dwordx4 v20, s[52:55], s66 offen lds\n"
        "v_perm_b32 v180, v89, v88, s64\n"
        "v_perm_b32 v181, v91, v90, s64\n"
        "v_perm_b32 v182, v93, v92, s64\n"
        "v_perm_b32 v183, v95, v94, s64\n"
        "s_add_u32 s66, s70, 671744\n"
        "s_add_u32 m0, s60, 31744\n"
        "s_nop 0\n"
        "buffer_load_dwordx4 v20, s[52:55], s66 offen lds\n"
        "v_perm_b32 v184, v81, v80, s65\n"
        "v_perm_b32 v185, v83, v82, s65\n"
        "v_perm_b32 v186, v85, v84, s65\n"
        "v_perm_b32 v187, v87, v86, s65\n"
        "v_perm_b32 v188, v89, v88, s65\n"
        "v_perm_b32 v189, v91, v90, s65\n"
        "v_perm_b32 v190, v93, v92, s65\n"
        "v_perm_b32 v191, v95, v94, s65\n"
        "s_waitcnt vmcnt(8)\n"
        "s_barrier\n"
        "ds_read2st64_b32 v[64:65], v22 offset0:64 offset1:66\n"
        "ds_read2st64_b32 v[66:67], v22 offset0:68 offset1:70\n"
        "ds_read2st64_b32 v[68:69], v22 offset0:72 offset1:74\n"
        "ds_read2st64_b32 v[70:71], v22 offset0:76 offset1:78\n"
        "ds_read2st64_b32 v[72:73], v22 offset0:80 offset1:82\n"
        "ds_read2st64_b32 v[74:75], v22 offset0:84 offset1:86\n"
        "ds_read2st64_b32 v[76:77], v22 offset0:88 offset1:90\n"
        "ds_read2st64_b32 v[78:79], v22 offset0:92 offset1:94\n"
        "ds_read2st64_b32 v[80:81], v52 offset0:64 offset1:66\n"
        "ds_read2st64_b32 v[82:83], v52 offset0:68 offset1:70\n"
        "ds_read2st64_b32 v[84:85], v52 offset0:72 offset1:74\n"
        "ds_read2st64_b32 v[86:87], v52 offset0:76 offset1:78\n"
        "ds_read2st64_b32 v[88:89], v52 offset0:80 offset1:82\n"
        "ds_read2st64_b32 v[90:91], v52 offset0:84 offset1:86\n"
        "ds_read2st64_b32 v[92:93], v52 offset0:88 offset1:90\n"
        "ds_read2st64_b32 v[94:95], v52 offset0:92 offset1:94\n"
        "s_add_u32 s66, s70, 32768\n"
        "s_add_u32 m0, s91, 0\n"
        "s_nop 0\n"
        "buffer_load_dwordx4 v20, s[52:55], s66 offen lds\n"
        "s_add_u32 s66, s70, 163840\n"
        "s_add_u32 m0, s91, 1024\n"
        "s_nop 0\n"
        "buffer_load_dwordx4 v20, s[52:55], s66 offen lds\n"
        "s_waitcnt lgkmcnt(8)\n"
        "v_perm_b32 v28, v65, v64, s64\n"
        "v_accvgpr_write_b32 a208, v28\n"
        "v_perm_b32 v29, v67, v66, s64\n"
        "v_accvgpr_write_b32 a209, v29\n"
        "v_perm_b32 v30, v69, v68, s64\n"
        "v_accvgpr_write_b32 a210, v30\n"
        "v_perm_b32 v31, v71, v70, s64\n"
        "v_accvgpr_write_b32 a211, v31\n"
        "s_add_u32 s66, s70, 49152\n"
        "s_add_u32 m0, s91, 2048\n"
        "s_nop 0\n"
        "buffer_load_dwordx4 v20, s[52:55], s66 offen lds\n"
        "v_perm_b32 v32, v73, v72, s64\n"
        "v_accvgpr_write_b32 a212, v32\n"
        "v_perm_b32 v33, v75, v74, s64\n"
        "v_accvgpr_write_b32 a213, v33\n"
        "v_perm_b32 v34, v77, v76, s64\n"
        "v_accvgpr_write_b32 a214, v34\n"
        "v_perm_b32 v35, v79, v78, s64\n"
        "v_accvgpr_write_b32 a215, v35\n"
        "s_add_u32 s66, s70, 180224\n"
        "s_add_u32 m0, s91, 3072\n"
        "s_nop 0\n"
        "buffer_load_dwordx4 v20, s[52:55], s66 offen lds\n"
        "v_perm_b32 v28, v65, v64, s65\n"
        "v_accvgpr_write_b32 a216, v28\n"
        "v_perm_b32 v29, v67, v66, s65\n"
        "v_accvgpr_write_b32 a217, v29\n"
        "v_perm_b32 v30, v69, v68, s65\n"
        "v_accvgpr_write_b32 a218, v30\n"
        "v_perm_b32 v31, v71, v70, s65\n"
        "v_accvgpr_write_b32 a219, v31\n"
        "s_add_u32 s66, s70, 557056\n"
        "s_add_u32 m0, s91, 4096\n"
        "s_nop 0\n"
        "buffer_load_dwordx4 v20, s[52:55], s66 offen lds\n"
        "v_perm_b32 v32, v73, v72, s65\n"
        "v_accvgpr_write_b32 a220, v32\n"
        "v_perm_b32 v33, v75, v74, s65\n"
        "v_accvgpr_write_b32 a221, v33\n"
        "v_perm_b32 v34, v77, v76, s65\n"
        "v_accvgpr_write_b32 a222, v34\n"
        "v_perm_b32 v35, v79, v78, s65\n"
        "v_accvgpr_write_b32 a223, v35\n"
        "s_add_u32 s66, s70, 688128\n"
        "s_add_u32 m0, s91, 5120\n"
        "s_nop 0\n"
        "buffer_load_dwordx4 v20, s[52:55], s66 offen lds\n"
        "s_waitcnt lgkmcnt(0)\n"
        "v_perm_b32 v28, v81, v80, s64\n"
        "v_accvgpr_write_b32 a240, v28\n"
        "v_perm_b32 v29, v83, v82, s64\n"
        "v_accvgpr_write_b32 a241, v29\n"
        "v_perm_b32 v30, v85, v84, s64\n"
        "v_accvgpr_write_b32 a242, v30\n"
        "v_perm_b32 v31, v87, v86, s64\n"
        "v_accvgpr_write_b32 a243, v31\n"
        "s_add_u32 s66, s70, 573440\n"
        "s_add_u32 m0, s91, 6144\n"
        "s_nop 0\n"
        "buffer_load_dwordx4 v20, s[52:55], s66 offen lds\n"
        "v_perm_b32 v32, v89, v88, s64\n"
        "v_accvgpr_write_b32 a244, v32\n"
        "v_perm_b32 v33, v91, v90, s64\n"
        "v_accvgpr_write_b32 a245, v33\n"
        "v_perm_b32 v34, v93, v92, s64\n"
        "v_accvgpr_write_b32 a246, v34\n"
        "v_perm_b32 v35, v95, v94, s64\n"
        "v_accvgpr_write_b32 a247, v35\n"
        "s_add_u32 s66, s70, 704512\n"
        "s_add_u32 m0, s91, 7168\n"
        "s_nop 0\n"
        "buffer_load_dwordx4 v20, s[52:55], s66 offen lds\n"
        "v_perm_b32 v28, v81, v80, s65\n"
        "v_accvgpr_write_b32 a248, v28\n"
        "v_perm_b32 v29, v83, v82, s65\n"
        "v_accvgpr_write_b32 a249, v29\n"
        "v_perm_b32 v30, v85, v84, s65\n"
        "v_accvgpr_write_b32 a250, v30\n"
        "v_perm_b32 v31, v87, v86, s65\n"
        "v_accvgpr_write_b32 a251, v31\n"
        "v_perm_b32 v32, v89, v88, s65\n"
        "v_accvgpr_write_b32 a252, v32\n"
        "v_perm_b32 v33, v91, v90, s65\n"
        "v_accvgpr_write_b32 a253, v33\n"
        "v_perm_b32 v34, v93, v92, s65\n"
        "v_accvgpr_write_b32 a254, v34\n"
        "v_perm_b32 v35, v95, v94, s65\n"
        "v_accvgpr_write_b32 a255, v35\n"
        "s_waitcnt vmcnt(8)\n"
        "s_barrier\n"
        "ds_read2st64_b32 v[64:65], v22 offset0:96 offset1:98\n"
        "ds_read2st64_b32 v[66:67], v22 offset0:100 offset1:102\n"
        "ds_read2st64_b32 v[68:69], v22 offset0:104 offset1:106\n"
        "ds_read2st64_b32 v[70:71], v22 offset0:108 offset1:110\n"
        "ds_read2st64_b32 v[72:73], v22 offset0:112 offset1:114\n"
        "ds_read2st64_b32 v[74:75], v22 offset0:116 offset1:118\n"
        "ds_read2st64_b32 v[76:77], v22 offset0:120 offset1:122\n"
        "ds_read2st64_b32 v[78:79], v22 offset0:124 offset1:126\n"
        "ds_read2st64_b32 v[80:81], v52 offset0:96 offset1:98\n"
        "ds_read2st64_b32 v[82:83], v52 offset0:100 offset1:102\n"
        "ds_read2st64_b32 v[84:85], v52 offset0:104 offset1:106\n"
        "ds_read2st64_b32 v[86:87], v52 offset0:108 offset1:110\n"
        "ds_read2st64_b32 v[88:89], v52 offset0:112 offset1:114\n"
        "ds_read2st64_b32 v[90:91], v52 offset0:116 offset1:118\n"
        "ds_read2st64_b32 v[92:93], v52 offset0:120 offset1:122\n"
        "ds_read2st64_b32 v[94:95], v52 offset0:124 offset1:126\n"
        "s_branch LR_f1\n"
        "LR_f0:\n"
        "s_sleep 2\n"
        "s_mov_b32 s66, 4222976\n"
        "buffer_load_dword v124, v125, s[40:43], s66 offen sc1\n"
        "s_add_u32 s81, s81, 1\n"
        "s_cmp_gt_u32 s81, 0x40000\n"
        "s_cbranch_scc1 LR_abort\n"
        "s_waitcnt vmcnt(0)\n"
        "LR_f1:\n"
        "v_cmp_eq_u32 vcc, 0, v124\n"
        "s_nop 0\n"
        "s_cbranch_vccnz LR_f0\n"
        "s_mov_b32 s76, 2097152\n"
        "buffer_load_dwordx4 v[96:99], v1, s[40:43], s76 offen sc1\n"
        "buffer_load_dwordx4 v[100:103], v1, s[40:43], s76 offen offset:1024 sc1\n"
        "buffer_load_dword v19, off, s[40:43], s62 sc1\n"
        "s_mov_b32 s66, 4202496\n"
        "buffer_load_dwordx4 v[120:123], off, s[40:43], s66 sc1\n"
        "buffer_load_dword v126, off, s[40:43], s66 offset:16 sc1\n"
        "s_waitcnt lgkmcnt(8)\n"
        "v_perm_b32 v192, v65, v64, s64\n"
        "v_perm_b32 v193, v67, v66, s64\n"
        "v_perm_b32 v194, v69, v68, s64\n"
        "v_perm_b32 v195, v71, v70, s64\n"
        "v_perm_b32 v196, v73, v72, s64\n"
        "v_perm_b32 v197, v75, v74, s64\n"
        "v_perm_b32 v198, v77, v76, s64\n"
        "v_perm_b32 v199, v79, v78, s64\n"
        "v_perm_b32 v200, v65, v64, s65\n"
        "v_perm_b32 v201, v67, v66, s65\n"
        "v_perm_b32 v202, v69, v68, s65\n"
        "v_perm_b32 v203, v71, v70, s65\n"
        "v_perm_b32 v204, v73, v72, s65\n"
        "v_perm_b32 v205, v75, v74, s65\n"
        "v_perm_b32 v206, v77, v76, s65\n"
        "v_perm_b32 v207, v79, v78, s65\n"
        "s_waitcnt lgkmcnt(0)\n"
        "v_perm_b32 v224, v81, v80, s64\n"
        "v_perm_b32 v225, v83, v82, s64\n"
        "v_perm_b32 v226, v85, v84, s64\n"
        "v_perm_b32 v227, v87, v86, s64\n"
        "v_perm_b32 v228, v89, v88, s64\n"
        "v_perm_b32 v229, v91, v90, s64\n"
        "v_perm_b32 v230, v93, v92, s64\n"
        "v_perm_b32 v231, v95, v94, s64\n"
        "v_perm_b32 v232, v81, v80, s65\n"
        "v_perm_b32 v233, v83, v82, s65\n"
        "v_perm_b32 v234, v85, v84, s65\n"
        "v_perm_b32 v235, v87, v86, s65\n"
        "v_perm_b32 v236, v89, v88, s65\n"
        "v_perm_b32 v237, v91, v90, s65\n"
        "v_perm_b32 v238, v93, v92, s65\n"
        "v_perm_b32 v239, v95, v94, s65\n"
        "s_waitcnt vmcnt(5)\n"
        "s_barrier\n"
        "ds_read2st64_b32 v[64:65], v23 offset0:0 offset1:2\n"
        "ds_read2st64_b32 v[66:67], v23 offset0:4 offset1:6\n"
        "ds_read2st64_b32 v[68:69], v23 offset0:8 offset1:10\n"
        "ds_read2st64_b32 v[70:71], v23 offset0:12 offset1:14\n"
        "ds_read2st64_b32 v[72:73], v23 offset0:16 offset1:18\n"
        "ds_read2st64_b32 v[74:75], v23 offset0:20 offset1:22\n"
        "ds_read2st64_b32 v[76:77], v23 offset0:24 offset1:26\n"
        "ds_read2st64_b32 v[78:79], v23 offset0:28 offset1:30\n"
        "ds_read2st64_b32 v[80:81], v53 offset0:0 offset1:2\n"
        "ds_read2st64_b32 v[82:83], v53 offset0:4 offset1:6\n"
        "ds_read2st64_b32 v[84:85], v53 offset0:8 offset1:10\n"
        "ds_read2st64_b32 v[86:87], v53 offset0:12 offset1:14\n"
        "ds_read2st64_b32 v[88:89], v53 offset0:16 offset1:18\n"
        "ds_read2st64_b32 v[90:91], v53 offset0:20 offset1:22\n"
        "ds_read2st64_b32 v[92:93], v53 offset0:24 offset1:26\n"
        "ds_read2st64_b32 v[94:95], v53 offset0:28 offset1:30\n"
        "s_waitcnt lgkmcnt(8)\n"
        "v_perm_b32 v208, v65, v64, s64\n"
        "v_perm_b32 v209, v67, v66, s64\n"
        "v_perm_b32 v210, v69, v68, s64\n"
        "v_perm_b32 v211, v71, v70, s64\n"
        "v_perm_b32 v212, v73, v72, s64\n"
        "v_perm_b32 v213, v75, v74, s64\n"
        "v_perm_b32 v214, v77, v76, s64\n"
        "v_perm_b32 v215, v79, v78, s64\n"
        "v_perm_b32 v216, v65, v64, s65\n"
        "v_perm_b32 v217, v67, v66, s65\n"
        "v_perm_b32 v218, v69, v68, s65\n"
        "v_perm_b32 v219, v71, v70, s65\n"
        "v_perm_b32 v220, v73, v72, s65\n"
        "v_perm_b32 v221, v75, v74, s65\n"
        "v_perm_b32 v222, v77, v76, s65\n"
        "v_perm_b32 v223, v79, v78, s65\n"
        "s_waitcnt lgkmcnt(0)\n"
        "v_perm_b32 v240, v81, v80, s64\n"
        "v_perm_b32 v241, v83, v82, s64\n"
        "v_perm_b32 v242, v85, v84, s64\n"
        "v_perm_b32 v243, v87, v86, s64\n"
        "v_perm_b32 v244, v89, v88, s64\n"
        "v_perm_b32 v245, v91, v90, s64\n"
        "v_perm_b32 v246, v93, v92, s64\n"
        "v_perm_b32 v247, v95, v94, s64\n"
        "v_perm_b32 v248, v81, v80, s65\n"
        "v_perm_b32 v249, v83, v82, s65\n"
        "v_perm_b32 v250, v85, v84, s65\n"
        "v_perm_b32 v251, v87, v86, s65\n"
        "v_perm_b32 v252, v89, v88, s65\n"
        "v_perm_b32 v253, v91, v90, s65\n"
        "v_perm_b32 v254, v93, v92, s65\n"
        "v_perm_b32 v255, v95, v94, s65\n"
        "s_waitcnt lgkmcnt(0)\n"
        "s_barrier\n"
        "ds_write_b128 v1, v[24:27] offset:16384\n"
        "s_mov_b32 s72, 0\n"
        "s_mov_b32 s76, 2097152\n"
        "s_and_b32 s66, s62, 0xfff\n"
        "s_mul_i32 s66, s66, 4160\n"
        "s_lshl_b32 s67, s61, 10\n"
        "s_add_u32 s66, s66, s67\n"
        "s_add_u32 s77, s66, 35651584\n"
        "s_add_u32 s78, s66, 35655680\n"
        "s_mov_b32 s79, 54525952\n"
        "s_mov_b32 s80, 0\n"
        "s_mov_b32 s81, 0\n"
        "s_mov_b32 s82, 0x3f803f80\n"
        "s_mov_b32 s83, 0x3f803f80\n"
        "s_mov_b32 s84, 0x00ff00ff\n"
        "s_mov_b32 s85, 0x00ff00ff\n"
        "s_mov_b32 s86, 0x3e0293ee\n"
        "s_mov_b32 s87, 0x3e0293ee\n"
        "ds_read_b128 v[104:107], v0 offset:0\n"
        "ds_read_b128 v[108:111], v0 offset:1024\n"
        "ds_read_b128 v[112:115], v0 offset:2048\n"
        "ds_read_b128 v[116:119], v0 offset:3072\n"
        "ds_read_b128 v[64:67], v0 offset:4096\n"
        "ds_read_b128 v[68:71], v0 offset:5120\n"
        "ds_read_b128 v[72:75], v0 offset:6144\n"
        "ds_read_b128 v[76:79], v0 offset:7168\n"
        "ds_read_b128 v[80:83], v0 offset:8192\n"
        "ds_read_b128 v[84:87], v0 offset:9216\n"
        "ds_read_b128 v[88:91], v0 offset:10240\n"
        "ds_read_b128 v[92:95], v0 offset:11264\n"
        "s_branch LR_kv1\n"
        "LR_kv0:\n"
        "s_sleep 2\n"
        "buffer_load_dword v19, off, s[40:43], s62 sc1\n"
        "s_mov_b32 s66, 4202496\n"
        "buffer_load_dwordx4 v[120:123], off, s[40:43], s66 sc1\n"
        "buffer_load_dword v126, off, s[40:43], s66 offset:16 sc1\n"
        "s_add_u32 s81, s81, 1\n"
        "s_cmp_gt_u32 s81, 0x40000\n"
        "s_cbranch_scc1 LR_abort\n"
        "LR_kv1:\n"
        "s_waitcnt vmcnt(0)\n"
        "v_readfirstlane_b32 s80, v19\n"
        "v_min3_u32 v124, v120, v121, v122\n"
        "v_min_u32 v124, v124, v123\n"
        "v_or_b32 v120, v120, v121\n"
        "v_or_b32 v122, v122, v123\n"
        "s_nop 0\n"
        "v_readfirstlane_b32 s66, v120\n"
        "v_readfirstlane_b32 s60, v122\n"
        "v_readfirstlane_b32 s67, v124\n"
        "s_nop 1\n"
        "s_cmp_lt_u32 s80, 1\n"
        "s_cbranch_scc1 LR_kv0\n"
        "s_cmp_eq_u32 s67, 0\n"
        "s_cbranch_scc1 LR_kv0\n"
        "s_mov_b32 s35, s66\n"
        "s_lshr_b32 s67, s39, 3\n"
        "s_and_b32 s67, s67, 1\n"
        "s_mul_i32 s67, s67, 10\n"
        "s_xor_b32 s60, s60, s67\n"
        "s_lshr_b32 s67, s39, 3\n"
        "s_and_b32 s67, s67, 2\n"
        "s_lshr_b32 s75, s66, s67\n"
        "s_and_b32 s75, s75, 3\n"
        "s_lshl_b32 s75, s75, 13\n"
        "s_lshl_b32 s52, s67, 18\n"
        "s_add_u32 s75, s75, s52\n"
        "s_lshr_b32 s67, s39, 4\n"
        "s_and_b32 s67, s67, 2\n"
        "s_lshr_b32 s73, s66, s67\n"
        "s_and_b32 s73, s73, 3\n"
        "s_lshl_b32 s73, s73, 13\n"
        "s_lshl_b32 s52, s67, 18\n"
        "s_add_u32 s73, s73, s52\n"
        "s_and_b32 s67, s61, 1\n"
        "s_lshl_b32 s67, s67, 14\n"
        "s_xor_b32 s75, s75, s67\n"
        "s_and_b32 s67, s39, 8\n"
        "s_lshl_b32 s67, s67, 11\n"
        "s_xor_b32 s73, s73, s67\n"
        "s_waitcnt lgkmcnt(0)\n"
        "buffer_load_dwordx4 v[20:23], v9, s[40:43], s77 offen sc1\n"
        "buffer_load_dwordx4 v[24:27], v9, s[40:43], s78 offen sc1\n"
        "buffer_load_dwordx4 v[28:31], v15, s[40:43], s79 offen sc1\n"
        "buffer_load_dwordx4 v[32:35], v15, s[40:43], s79 offen offset:1024 sc1\n"
        "buffer_load_dword v19, off, s[40:43], s62 sc1\n"
        "buffer_load_dword v18, off, s[40:43], s62 sc1\n"
        "buffer_load_dword v18, off, s[40:43], s62 sc1\n"
        "s_add_u32 s77, s77, 8192\n"
        "s_add_u32 s78, s78, 8192\n"
        "s_add_u32 s79, s79, 8192\n"
        "v_mov_b64 v[36:37], 0\n"
        "v_mov_b64 v[38:39], 0\n"
        "v_mov_b64 v[40:41], 0\n"
        "v_mov_b64 v[42:43], 0\n"
        "v_mov_b64 v[44:45], 0\n"
        "v_mov_b64 v[46:47], 0\n"
        "v_mov_b64 v[48:49], 0\n"
        "v_mov_b64 v[50:51], 0\n"
        "v_mov_b64 v[52:53], 0\n"
        "v_mov_b64 v[54:55], 0\n"
        "v_mov_b64 v[56:57], 0\n"
        "v_mov_b64 v[58:59], 0\n"
        "s_getpc_b64 s[44:45]\n"
        "LR_slBp:\n"
        "s_add_u32 s44, s44, LR_slB-LR_slBp+48\n"
        "s_addc_u32 s45, s45, 0\n"
        "s_getpc_b64 s[46:47]\n"
        "LR_slAp:\n"
        "s_add_u32 s46, s46, LR_slA-LR_slAp+48\n"
        "s_addc_u32 s47, s47, 0\n"
        "s_mov_b32 s68, 0\n"
        "s_mov_b32 s52, 0\n"
        "s_mov_b32 s69, 0\n"
        "s_mov_b32 s70, 0\n"
        "s_mov_b32 s65, 0\n"
        "s_mov_b32 s71, 176\n"
        "s_mov_b32 s64, 52\n"
        "s_lshr_b32 s66, s72, 3\n"
        "s_and_b32 s66, s66, 2\n"
        "s_lshr_b32 s66, s35, s66\n"
        "s_lshr_b32 s67, s72, 1\n"
        "s_and_b32 s67, s67, 2\n"
        "s_xor_b32 s66, s66, s67\n"
        "s_xor_b32 s66, s66, 2\n"
        "s_and_b32 s66, s66, 3\n"
        "s_lshl_b32 s66, s66, 13\n"
        "s_lshl_b32 s95, s72, 15\n"
        "s_add_u32 s95, s95, s66\n"
        "v_readfirstlane_b32 s97, v126\n"
        "s_nop 1\n"
        "s_mul_i32 s97, s97, 6\n"
        "s_mov_b32 s63, 2097152\n"
        "s_cmp_lt_u32 s61, 2\n"
        "s_cselect_b32 s36, 203, 12\n"
        "s_mov_b32 s90, 256\n"
        "s_mov_b32 s96, 2\n"
        "s_mov_b32 s91, 0\n"
        "s_mov_b32 s74, 0\n"
        "s_mov_b32 s38, 20\n"
        "s_branch LR_retA\n"
        ".p2align 6\n"
        "LR_top:\n"
        "s_waitcnt vmcnt(4)\n"
        "LR_retA:\n"
        "ds_write_b128 v1, v[96:99] offset:0\n"
        "v_smfmac_f32_16x16x64_bf16 v[36:39], v[96:99], v[104:111], v16\n"
        "s_waitcnt vmcnt(3)\n"
        "ds_write_b128 v1, v[100:103] offset:1024\n"
        "v_max3_u32 v60, v96, v97, v98\n"
        "v_smfmac_f32_16x16x64_bf16 v[40:43], v[96:99], v[112:119], v16\n"
        "v_max3_u32 v61, v99, v100, v101\n"
        "v_smfmac_f32_16x16x64_bf16 v[44:47], v[96:99], v[64:71], v16\n"
        "v_max3_u32 v60, v60, v102, v103\n"
        "v_smfmac_f32_16x16x64_bf16 v[48:51], v[96:99], v[72:79], v16\n"
        "v_max_u32 v60, v60, v61\n"
        "v_smfmac_f32_16x16x64_bf16 v[36:39], v[100:103], v[80:87], v16\n"
        "v_cmp_eq_u32 vcc, -1, v60\n"
        "v_smfmac_f32_16x16x64_bf16 v[40:43], v[100:103], v[88:95], v16\n"
        "v_smfmac_f32_16x16x64_bf16 v[44:47], v[100:103], a[160:167], v16\n"
        "s_cbranch_vccnz LR_slowA\n"
        "v_smfmac_f32_16x16x64_bf16 v[48:51], v[100:103], a[168:175], v16\n"
        "s_waitcnt lgkmcnt(0)\n"
        "s_barrier\n"
        "ds_read_b128 v[104:107], v2 offset:0\n"
        "ds_read_b128 v[108:111], v2 offset:1024\n"
        "v_smfmac_f32_16x16x64_bf16 v[52:55], v[96:99], v[128:135], v16\n"
        "ds_read_b128 v[112:115], v3 offset:0\n"
        "v_smfmac_f32_16x16x64_bf16 v[56:59], v[96:99], v[136:143], v16\n"
        "ds_read_b128 v[116:119], v3 offset:1024\n"
        "v_smfmac_f32_16x16x64_bf16 v[52:55], v[100:103], v[144:151], v16\n"
        "ds_read_b128 v[120:123], v4 offset:0\n"
        "v_smfmac_f32_16x16x64_bf16 v[56:59], v[100:103], v[152:159], v16\n"
        "ds_read_b128 v[124:127], v4 offset:1024\n"
        "s_waitcnt lgkmcnt(5)\n"
        "v_smfmac_f32_16x16x64_bf16 v[36:39], v[104:107], a[64:71], v16\n"
        "ds_read_b128 v[64:67], v0 offset:12288\n"
        "s_waitcnt vmcnt(2)\n"
        "v_readfirstlane_b32 s67, v19\n"
        "v_smfmac_f32_16x16x64_bf16 v[40:43], v[104:107], a[72:79], v16\n"
        "ds_read_b128 v[68:71], v0 offset:13312\n"
        "s_waitcnt lgkmcnt(6)\n"
        "v_smfmac_f32_16x16x64_bf16 v[36:39], v[108:111], a[80:87], v16\n"
        "ds_read_b128 v[72:75], v0 offset:14336\n"
        "s_cmp_lt_u32 s67, s96\n"
        "v_smfmac_f32_16x16x64_bf16 v[40:43], v[108:111], a[88:95], v16\n"
        "ds_read_b128 v[76:79], v0 offset:15360\n"
        "s_waitcnt lgkmcnt(7)\n"
        "v_smfmac_f32_16x16x64_bf16 v[36:39], v[112:115], a[96:103], v16\n"
        "s_cbranch_scc1 LR_slowKV\n"
        "LR_retKV:\n"
        "v_mov_b32 v18, s91\n"
        "v_smfmac_f32_16x16x64_bf16 v[40:43], v[112:115], a[104:111], v16\n"
        "s_waitcnt lgkmcnt(6)\n"
        "v_mov_b32 v19, s74\n"
        "v_smfmac_f32_16x16x64_bf16 v[36:39], v[116:119], a[112:119], v16\n"
        "buffer_store_dwordx2 v[18:19], v17, s[40:43], s95 offen sc1\n"
        "v_mov_b64 v[96:97], s[82:83]\n"
        "v_smfmac_f32_16x16x64_bf16 v[40:43], v[116:119], a[120:127], v16\n"
        "s_waitcnt lgkmcnt(5)\n"
        "v_mov_b64 v[98:99], s[82:83]\n"
        "v_smfmac_f32_16x16x64_bf16 v[36:39], v[120:123], a[128:135], v16\n"
        "v_mov_b64 v[100:101], 0\n"
        "v_smfmac_f32_16x16x64_bf16 v[40:43], v[120:123], a[136:143], v16\n"
        "s_waitcnt lgkmcnt(4)\n"
        "v_mov_b64 v[102:103], 0\n"
        "v_smfmac_f32_16x16x64_bf16 v[36:39], v[124:127], a[144:151], v16\n"
        "buffer_load_dwordx4 v[80:83], v9, s[40:43], s77 offen sc1\n"
        "v_smfmac_f32_16x16x64_bf16 v[40:43], v[124:127], a[152:159], v16\n"
        "s_waitcnt lgkmcnt(0)\n"
        "v_smfmac_f32_16x16x64_bf16 v[44:47], v[104:107], v[64:71], v16\n"
        "buffer_load_dwordx4 v[84:87], v9, s[40:43], s78 offen sc1\n"
        "v_smfmac_f32_16x16x64_bf16 v[48:51], v[104:107], v[72:79], v16\n"
        "v_add_f32 v36, v36, v38\n"
        "v_add_f32 v37, v37, v39\n"
        "v_smfmac_f32_16x16x64_bf16 v[44:47], v[108:111], a[176:183], v16\n"
        "v_add_f32 v40, v40, v42\n"
        "v_add_f32 v41, v41, v43\n"
        "v_smfmac_f32_16x16x64_bf16 v[48:51], v[108:111], a[184:191], v16\n"
        "v_cvt_pk_bf16_f32 v36, v36, v40\n"
        "v_cvt_pk_bf16_f32 v37, v37, v41\n"
        "v_smfmac_f32_16x16x64_bf16 v[44:47], v[112:115], a[192:199], v16\n"
        "ds_write2_b32 v5, v36, v37 offset0:0 offset1:4\n"
        "ds_read_b128 v[60:63], v6 offset:8192\n"
        "v_smfmac_f32_16x16x64_bf16 v[48:51], v[112:115], a[200:207], v16\n"
        "v_smfmac_f32_16x16x64_bf16 v[44:47], v[116:119], a[208:215], v16\n"
        "buffer_load_dwordx4 v[88:91], v15, s[40:43], s79 offen sc1\n"
        "v_smfmac_f32_16x16x64_bf16 v[48:51], v[116:119], a[216:223], v16\n"
        "v_smfmac_f32_16x16x64_bf16 v[44:47], v[120:123], a[224:231], v16\n"
        "v_smfmac_f32_16x16x64_bf16 v[48:51], v[120:123], a[232:239], v16\n"
        "s_waitcnt lgkmcnt(1)\n"
        "s_barrier\n"
        "v_smfmac_f32_16x16x64_bf16 v[44:47], v[124:127], a[240:247], v16\n"
        "ds_read_b128 v[64:67], v7 offset:8192\n"
        "s_waitcnt lgkmcnt(1)\n"
        "v_mfma_f32_16x16x32_bf16 v[36:39], v[20:23], v[60:63], 0\n"
        "ds_read_b128 v[68:71], v7 offset:8704\n"
        "v_mfma_f32_16x16x32_bf16 v[40:43], v[24:27], v[60:63], 0\n"
        "ds_read_b128 v[72:75], v7 offset:9216\n"
        "v_smfmac_f32_16x16x64_bf16 v[48:51], v[124:127], a[248:255], v16\n"
        "ds_read_b128 v[76:79], v7 offset:9728\n"
        "v_smfmac_f32_16x16x64_bf16 v[52:55], v[104:107], v[160:167], v16\n"
        "buffer_load_dwordx4 v[92:95], v15, s[40:43], s79 offen offset:1024 sc1\n"
        "v_smfmac_f32_16x16x64_bf16 v[56:59], v[104:107], v[168:175], v16\n"
        "v_cndmask_b32 v36, v40, v36, s[84:85]\n"
        "v_cndmask_b32 v37, v41, v37, s[84:85]\n"
        "v_cndmask_b32 v38, v42, v38, s[84:85]\n"
        "v_cndmask_b32 v39, v43, v39, s[84:85]\n"
        "v_smfmac_f32_16x16x64_bf16 v[52:55], v[108:111], v[176:183], v16\n"
        "ds_write_b128 v1, v[36:39] offset:0\n"
        "v_add_f32 v44, v44, v46\n"
        "v_add_f32 v45, v45, v47\n"
        "v_add_f32 v48, v48, v50\n"
        "v_add_f32 v49, v49, v51\n"
        "v_cvt_pk_bf16_f32 v48, v44, v48\n"
        "v_cvt_pk_bf16_f32 v49, v45, v49\n"
        "v_smfmac_f32_16x16x64_bf16 v[56:59], v[108:111], v[184:191], v16\n"
        "ds_write2_b32 v8, v48, v49 offset0:0 offset1:4\n"
        "v_mov_b64 v[44:45], 0\n"
        "v_smfmac_f32_16x16x64_bf16 v[52:55], v[112:115], v[192:199], v16\n"
        "v_mov_b64 v[46:47], 0\n"
        "v_smfmac_f32_16x16x64_bf16 v[56:59], v[112:115], v[200:207], v16\n"
        "v_smfmac_f32_16x16x64_bf16 v[52:55], v[116:119], v[208:215], v16\n"
        "v_smfmac_f32_16x16x64_bf16 v[56:59], v[116:119], v[216:223], v16\n"
        "s_waitcnt lgkmcnt(0)\n"
        "s_barrier\n"
        "ds_read_b128 v[40:43], v3 offset:0\n"
        "v_smfmac_f32_16x16x64_bf16 v[52:55], v[120:123], v[224:231], v16\n"
        "ds_read_b128 v[104:107], v2 offset:0\n"
        "ds_read_b128 v[108:111], v4 offset:0\n"
        "v_smfmac_f32_16x16x64_bf16 v[56:59], v[120:123], v[232:239], v16\n"
        "ds_read_b128 v[112:115], v9 offset:10240\n"
        "v_smfmac_f32_16x16x64_bf16 v[52:55], v[124:127], v[240:247], v16\n"
        "ds_read_b128 v[116:119], v9 offset:11264\n"
        "v_smfmac_f32_16x16x64_bf16 v[56:59], v[124:127], v[248:255], v16\n"
        "s_waitcnt lgkmcnt(2)\n"
        "v_add_f32 v36, v36, v40\n"
        "v_add_f32 v37, v37, v41\n"
        "v_add_f32 v38, v38, v42\n"
        "v_add_f32 v39, v39, v43\n"
        "v_pk_add_f32 v[104:105], v[104:105], v[108:109]\n"
        "v_pk_add_f32 v[106:107], v[106:107], v[110:111]\n"
        "v_pk_add_f32 v[36:37], v[36:37], v[104:105]\n"
        "v_pk_add_f32 v[38:39], v[38:39], v[106:107]\n"
        "v_pk_mul_f32 v[104:105], v[36:37], s[86:87]\n"
        "v_pk_mul_f32 v[106:107], v[38:39], s[86:87]\n"
        "v_pk_add_f32 v[108:109], v[52:53], v[54:55]\n"
        "v_pk_add_f32 v[110:111], v[56:57], v[58:59]\n"
        "s_waitcnt lgkmcnt(0)\n"
        "v_smfmac_f32_16x16x64_bf16 v[44:47], v[112:115], v[64:71], v16\n"
        "v_exp_f32 v104, v104\n"
        "v_smfmac_f32_16x16x64_bf16 v[44:47], v[116:119], v[72:79], v16\n"
        "v_exp_f32 v105, v105\n"
        "v_exp_f32 v106, v106\n"
        "v_exp_f32 v107, v107\n"
        "v_cvt_pk_bf16_f32 v60, v104, v105\n"
        "v_cvt_pk_bf16_f32 v61, v106, v107\n"
        "v_cvt_pk_bf16_f32 v108, v108, v109\n"
        "v_mov_b32_dpp v62, v60 row_ror:8 row_mask:0xf bank_mask:0xf\n"
        "v_mov_b32_dpp v63, v61 row_ror:8 row_mask:0xf bank_mask:0xf\n"
        "v_cvt_pk_bf16_f32 v109, v110, v111\n"
        "v_pk_add_f32 v[44:45], v[44:45], v[46:47]\n"
        "LR_retS:\n"
        "v_pk_mul_f32 v[46:47], v[44:45], s[86:87]\n"
        "v_mfma_f32_16x16x32_bf16 v[48:51], v[96:99], v[60:63], 0\n"
        "v_exp_f32 v46, v46\n"
        "v_mfma_f32_16x16x32_bf16 v[20:23], v[28:31], v[60:63], 0\n"
        "v_exp_f32 v47, v47\n"
        "v_mfma_f32_16x16x32_bf16 v[24:27], v[32:35], v[60:63], 0\n"
        "v_cvt_pk_bf16_f32 v100, v46, v47\n"
        "v_cvt_pk_bf16_f32 v103, v46, v47\n"
        "v_mov_b64 v[64:65], 0\n"
        "v_mfma_f32_16x16x16_bf16 v[48:51], v[96:97], v[100:101], v[48:51]\n"
        "v_mov_b64 v[66:67], 0\n"
        "v_mov_b64 v[68:69], 0\n"
        "v_mfma_f32_16x16x16_bf16 v[20:23], v[108:109], v[100:101], v[20:23]\n"
        "v_mov_b64 v[70:71], 0\n"
        "v_mov_b64 v[72:73], 0\n"
        "v_mfma_f32_16x16x16_bf16 v[24:27], v[108:109], v[102:103], v[24:27]\n"
        "s_mov_b64 exec, s[84:85]\n"
        "v_rcp_f32 v40, v48\n"
        "s_nop 0\n"
        "v_max_f32 v42, v40, v48\n"
        "v_cmp_lt_f32 vcc, 0x5d800000, v42\n"
        "v_mul_f32 v20, v20, v40\n"
        "v_mul_f32 v21, v21, v40\n"
        "v_mul_f32 v22, v22, v40\n"
        "v_mul_f32 v23, v23, v40\n"
        "v_pk_mul_f32 v[24:25], v[24:25], v[40:41] op_sel_hi:[1,0]\n"
        "v_pk_mul_f32 v[26:27], v[26:27], v[40:41] op_sel_hi:[1,0]\n"
        "v_cvt_pk_bf16_f32 v112, v20, v24\n"
        "v_cvt_pk_bf16_f32 v113, v21, v25\n"
        "v_cvt_pk_bf16_f32 v114, v22, v26\n"
        "v_cvt_pk_bf16_f32 v115, v23, v27\n"
        "ds_write_b128 v14, v[112:115] offset:12288\n"
        "s_cbranch_vccnz LR_slowS\n"
        "s_mov_b64 exec, -1\n"
        "buffer_load_dwordx2 v[100:101], v10, s[40:43], s95 offen sc1\n"
        "v_mov_b64 v[74:75], 0\n"
        "v_mov_b64 v[76:77], 0\n"
        "v_mov_b64 v[78:79], 0\n"
        "s_waitcnt lgkmcnt(0)\n"
        "s_barrier\n"
        "ds_read_b128 v[52:55], v9 offset:12288\n"
        "ds_read_b128 v[56:59], v9 offset:13312\n"
        "s_waitcnt lgkmcnt(1)\n"
        "v_smfmac_f32_16x16x64_bf16 v[64:67], v[52:55], a[0:7], v16\n"
        "s_waitcnt lgkmcnt(0)\n"
        "v_smfmac_f32_16x16x64_bf16 v[64:67], v[56:59], a[8:15], v16\n"
        "v_smfmac_f32_16x16x64_bf16 v[68:71], v[52:55], a[16:23], v16\n"
        "v_smfmac_f32_16x16x64_bf16 v[68:71], v[56:59], a[24:31], v16\n"
        "v_smfmac_f32_16x16x64_bf16 v[72:75], v[52:55], a[32:39], v16\n"
        "v_add_f32 v64, v64, v66\n"
        "v_smfmac_f32_16x16x64_bf16 v[72:75], v[56:59], a[40:47], v16\n"
        "v_add_f32 v65, v65, v67\n"
        "v_smfmac_f32_16x16x64_bf16 v[76:79], v[52:55], a[48:55], v16\n"
        "v_cvt_pk_bf16_f32 v60, v64, v65\n"
        "v_smfmac_f32_16x16x64_bf16 v[76:79], v[56:59], a[56:63], v16\n"
        "v_add_f32 v68, v68, v70\n"
        "v_add_f32 v69, v69, v71\n"
        "v_cvt_pk_bf16_f32 v61, v68, v69\n"
        "v_add_f32 v72, v72, v74\n"
        "v_add_f32 v73, v73, v75\n"
        "v_cvt_pk_bf16_f32 v62, v72, v73\n"
        "s_nop 0\n"
        "v_pk_add_f32 v[76:77], v[76:77], v[78:79]\n"
        "v_cvt_pk_bf16_f32 v63, v76, v77\n"
        "buffer_store_dwordx4 v[60:63], v11, s[40:43], s75 offen sc1\n"
        "s_memrealtime s[88:89]\n"
        "s_nop 2\n"
        "s_memrealtime s[54:55]\n"
        "s_nop 2\n"
        "s_memrealtime s[56:57]\n"
        "s_nop 2\n"
        "s_memrealtime s[58:59]\n"
        "s_nop 2\n"
        "s_memrealtime s[98:99]\n"
        "s_nop 2\n"
        "s_memrealtime s[48:49]\n"
        "s_waitcnt lgkmcnt(0)\n"
        "s_add_u32 s88, s88, s54\n"
        "s_add_u32 s56, s56, s58\n"
        "s_add_u32 s98, s98, s48\n"
        "s_add_u32 s88, s88, s56\n"
        "s_add_u32 s88, s88, s98\n"
        "s_sub_u32 s88, s88, s97\n"
        "s_lshr_b32 s91, s88, 2\n"
        "s_sub_i32 s66, s52, s91\n"
        "s_cmp_ge_i32 s66, 0\n"
        "s_cselect_b32 s66, s66, 35\n"
        "s_max_i32 s66, s66, 12\n"
        "s_min_i32 s66, s66, s36\n"
        "s_lshl_b32 s66, s66, 2\n"
        "s_sub_u32 s48, s44, s66\n"
        "s_subb_u32 s49, s45, 0\n"
        "s_setpc_b64 s[48:49]\n"
        "s_nop 3\n"
        "s_nop 3\n"
        "s_nop 3\n"
        "s_nop 3\n"
        "s_nop 3\n"
        "s_nop 3\n"
        "s_nop 3\n"
        "s_nop 3\n"
        "s_nop 3\n"
        "s_nop 3\n"
        "s_nop 3\n"
        "s_nop 3\n"
        "s_nop 3\n"
        "s_nop 3\n"
        "s_nop 3\n"
        "s_nop 3\n"
        "s_nop 3\n"
        "s_nop 3\n"
        "s_nop 3\n"
        "s_nop 3\n"
        "s_nop 3\n"
        "s_nop 3\n"
        "s_nop 3\n"
        "s_nop 3\n"
        "s_nop 3\n"
        "s_nop 3\n"
        "s_nop 3\n"
        "s_nop 3\n"
        "s_nop 3\n"
        "s_nop 3\n"
        "s_nop 3\n"
        "s_nop 3\n"
        "s_nop 3\n"
        "s_nop 3\n"
        "s_nop 3\n"
        "s_nop 3\n"
        "s_nop 3\n"
        "s_nop 3\n"
        "s_nop 3\n"
        "s_nop 3\n"
        "s_nop 3\n"
        "s_nop 3\n"
        "s_nop 3\n"
        "s_nop 3\n"
        "s_nop 3\n"
        "s_nop 3\n"
        "s_nop 3\n"
        "s_nop 3\n"
        "s_nop 3\n"
        "s_nop 3\n"
        "s_nop 3\n"
        "s_nop 3\n"
        "s_nop 3\n"
        "s_nop 3\n"
        "s_nop 3\n"
        "s_nop 3\n"
        "s_nop 3\n"
        "s_nop 3\n"
        "s_nop 3\n"
        "s_nop 3\n"
        "s_nop 3\n"
        "s_nop 3\n"
        "s_nop 3\n"
        "s_nop 3\n"
        "s_nop 3\n"
        "s_nop 3\n"
        "s_nop 3\n"
        "s_nop 3\n"
        "s_nop 3\n"
        "s_nop 3\n"
        "s_nop 3\n"
        "s_nop 3\n"
        "s_nop 3\n"
        "s_nop 3\n"
        "s_nop 3\n"
        "s_nop 3\n"
        "s_nop 3\n"
        "s_nop 3\n"
        "s_nop 3\n"
        "s_nop 3\n"
        "s_nop 3\n"
        "s_nop 3\n"
        "s_nop 3\n"
        "s_nop 3\n"
        "s_nop 3\n"
        "s_nop 3\n"
        "s_nop 3\n"
        "s_nop 3\n"
        "s_nop 3\n"
        "s_nop 3\n"
        "s_nop 3\n"
        "s_nop 3\n"
        "s_nop 3\n"
        "s_nop 3\n"
        "s_nop 3\n"
        "s_nop 3\n"
        "s_nop 3\n"
        "s_nop 3\n"
        "s_nop 3\n"
        "s_nop 3\n"
        "s_nop 3\n"
        "s_nop 3\n"
        "s_nop 3\n"
        "s_nop 3\n"
        "s_nop 3\n"
        "s_nop 3\n"
        "s_nop 3\n"
        "s_nop 3\n"
        "s_nop 3\n"
        "s_nop 3\n"
        "s_nop 3\n"
        "s_nop 3\n"
        "s_nop 3\n"
        "s_nop 3\n"
        "s_nop 3\n"
        "s_nop 3\n"
        "s_nop 3\n"
        "s_nop 3\n"
        "s_nop 3\n"
        "s_nop 3\n"
        "s_nop 3\n"
        "s_nop 3\n"
        "s_nop 3\n"
        "s_nop 3\n"
        "s_nop 3\n"
        "s_nop 3\n"
        "s_nop 3\n"
        "s_nop 3\n"
        "s_nop 3\n"
        "s_nop 3\n"
        "s_nop 3\n"
        "s_nop 3\n"
        "s_nop 3\n"
        "s_nop 3\n"
        "s_nop 3\n"
        "s_nop 3\n"
        "s_nop 3\n"
        "s_nop 3\n"
        "s_nop 3\n"
        "s_nop 3\n"
        "s_nop 3\n"
        "s_nop 3\n"
        "s_nop 3\n"
        "s_nop 3\n"
        "s_nop 3\n"
        "s_nop 3\n"
        "s_nop 3\n"
        "s_nop 3\n"
        "s_nop 3\n"
        "s_nop 3\n"
        "s_nop 3\n"
        "s_nop 3\n"
        "s_nop 3\n"
        "s_nop 3\n"
        "s_nop 3\n"
        "s_nop 3\n"
        "s_nop 3\n"
        "s_nop 3\n"
        "s_nop 3\n"
        "s_nop 3\n"
        "s_nop 3\n"
        "s_nop 3\n"
        "s_nop 3\n"
        "s_nop 3\n"
        "s_nop 3\n"
        "s_nop 3\n"
        "s_nop 3\n"
        "s_nop 3\n"
        "s_nop 3\n"
        "s_nop 3\n"
        "s_nop 3\n"
        "s_nop 3\n"
        "s_nop 3\n"
        "s_nop 3\n"
        "s_nop 3\n"
        "s_nop 3\n"
        "s_nop 3\n"
        "s_nop 3\n"
        "s_nop 3\n"
        "s_nop 3\n"
        "s_nop 3\n"
        "s_nop 3\n"
        "s_nop 3\n"
        "s_nop 3\n"
        "s_nop 3\n"
        "s_nop 3\n"
        "s_nop 3\n"
        "s_nop 3\n"
        "s_nop 3\n"
        "s_nop 3\n"
        "s_nop 3\n"
        "s_nop 3\n"
        "LR_slB:\n"
        "buffer_load_dwordx4 v[120:123], v12, s[40:43], s73 offen sc1\n"
        "buffer_load_dwordx4 v[124:127], v12, s[40:43], s73 offen offset:1024 sc1\n"
        "s_add_u32 s63, s63, 32768\n"
        "s_lshr_b32 s66, s63, 18\n"
        "s_and_b32 s66, s66, 2\n"
        "s_lshr_b32 s66, s60, s66\n"
        "s_and_b32 s66, s66, 3\n"
        "s_lshl_b32 s66, s66, 13\n"
        "s_add_u32 s76, s63, s66\n"
        "s_andn2_b32 s34, s76, 16384\n"
        "ds_read_b128 v[60:63], v1 offset:16384\n"
        "s_waitcnt vmcnt(4)\n"
        "v_mov_b64 v[20:21], v[80:81]\n"
        "v_mov_b64 v[22:23], v[82:83]\n"
        "v_mov_b64 v[24:25], v[84:85]\n"
        "v_mov_b64 v[26:27], v[86:87]\n"
        "v_mov_b64 v[28:29], v[88:89]\n"
        "v_mov_b64 v[30:31], v[90:91]\n"
        "v_mov_b64 v[32:33], v[92:93]\n"
        "v_mov_b64 v[34:35], v[94:95]\n"
        "s_add_u32 s77, s77, 8192\n"
        "s_add_u32 s78, s78, 8192\n"
        "s_add_u32 s79, s79, 8192\n"
        "s_cmp_lg_u32 s61, 2\n"
        "s_cbranch_scc1 LR_nsch\n"
        "s_waitcnt vmcnt(3)\n"
        "v_max_i32_dpp v100, v100, v100 row_ror:1 row_mask:0xf bank_mask:0xf\n"
        "v_max_i32_dpp v101, v101, v101 row_ror:1 row_mask:0xf bank_mask:0xf\n"
        "s_nop 0\n"
        "v_max_i32_dpp v100, v100, v100 row_ror:2 row_mask:0xf bank_mask:0xf\n"
        "v_max_i32_dpp v101, v101, v101 row_ror:2 row_mask:0xf bank_mask:0xf\n"
        "s_nop 0\n"
        "v_max_i32_dpp v100, v100, v100 row_ror:4 row_mask:0xf bank_mask:0xf\n"
        "v_max_i32_dpp v101, v101, v101 row_ror:4 row_mask:0xf bank_mask:0xf\n"
        "s_nop 0\n"
        "v_max_i32_dpp v100, v100, v100 row_ror:8 row_mask:0xf bank_mask:0xf\n"
        "v_max_i32_dpp v101, v101, v101 row_ror:8 row_mask:0xf bank_mask:0xf\n"
        "s_nop 0\n"
        "v_max_i32_dpp v100, v100, v100 row_bcast:15 row_mask:0xf bank_mask:0xf\n"
        "v_max_i32_dpp v101, v101, v101 row_bcast:15 row_mask:0xf bank_mask:0xf\n"
        "s_nop 0\n"
        "v_max_i32_dpp v100, v100, v100 row_bcast:31 row_mask:0xf bank_mask:0xf\n"
        "v_max_i32_dpp v101, v101, v101 row_bcast:31 row_mask:0xf bank_mask:0xf\n"
        "s_nop 0\n"
        "v_readlane_b32 s50, v100, 63\n"
        "v_readlane_b32 s51, v101, 63\n"
        "s_sub_i32 s53, s50, s69\n"
        "s_cmp_lg_u32 s65, 0\n"
        "s_cselect_b32 s66, s53, s71\n"
        "s_add_i32 s67, s71, s90\n"
        "s_add_i32 s67, s67, 2\n"
        "s_min_i32 s66, s66, s67\n"
        "s_sub_i32 s67, s71, 1\n"
        "s_max_i32 s71, s66, s67\n"
        "s_add_i32 s66, s66, 2\n"
        "s_min_i32 s71, s71, s66\n"
        "s_sub_i32 s53, s51, s70\n"
        "s_sub_i32 s66, s70, s50\n"
        "s_cmp_ge_i32 s66, 8\n"
        "s_cselect_b32 s66, s53, s64\n"
        "s_add_i32 s67, s64, s90\n"
        "s_add_i32 s67, s67, 2\n"
        "s_min_i32 s66, s66, s67\n"
        "s_sub_i32 s67, s64, 1\n"
        "s_max_i32 s64, s66, s67\n"
        "s_add_i32 s66, s66, 2\n"
        "s_min_i32 s64, s64, s66\n"
        "s_add_i32 s67, s51, s71\n"
        "s_add_i32 s67, s67, 48\n"
        "s_add_i32 s67, s67, s38\n"
        "s_lshl_b32 s53, s38, 1\n"
        "s_add_i32 s53, s53, 16\n"
        "s_min_i32 s53, s53, 150\n"
        "s_sub_i32 s66, s68, s51\n"
        "s_cmp_ge_i32 s66, 7\n"
        "s_cselect_b32 s67, s52, s67\n"
        "s_cselect_b32 s65, 1, 0\n"
        "s_cselect_b32 s38, 10, s53\n"
        "s_lshr_b32 s90, s90, 1\n"
        "s_mov_b32 s69, s68\n"
        "s_mov_b32 s70, s52\n"
        "s_add_i32 s68, s67, s64\n"
        "s_add_i32 s68, s68, 13\n"
        "s_add_i32 s52, s68, s71\n"
        "s_add_i32 s52, s52, 20\n"
        "v_mov_b32 v102, s68\n"
        "v_mov_b32 v103, s52\n"
        "ds_write_b64 v9, v[102:103] offset:17408\n"
        "LR_nsch:\n"
        "ds_read_b128 v[104:107], v0 offset:0\n"
        "ds_read_b128 v[108:111], v0 offset:1024\n"
        "ds_read_b128 v[112:115], v0 offset:2048\n"
        "ds_read_b128 v[116:119], v0 offset:3072\n"
        "ds_read_b128 v[64:67], v0 offset:4096\n"
        "ds_read_b128 v[68:71], v0 offset:5120\n"
        "ds_read_b128 v[72:75], v0 offset:6144\n"
        "ds_read_b128 v[76:79], v0 offset:7168\n"
        "ds_read_b128 v[80:83], v0 offset:8192\n"
        "ds_read_b128 v[84:87], v0 offset:9216\n"
        "ds_read_b128 v[88:91], v0 offset:10240\n"
        "ds_read_b128 v[92:95], v0 offset:11264\n"
        "v_mov_b64 v[44:45], 0\n"
        "v_mov_b64 v[46:47], 0\n"
        "v_mov_b64 v[48:49], 0\n"
        "v_mov_b64 v[50:51], 0\n"
        "v_mov_b64 v[52:53], 0\n"
        "v_mov_b64 v[54:55], 0\n"
        "v_mov_b64 v[56:57], 0\n"
        "v_mov_b64 v[58:59], 0\n"
        "v_mov_b64 v[36:37], 0\n"
        "v_mov_b64 v[38:39], 0\n"
        "v_mov_b64 v[40:41], 0\n"
        "v_mov_b64 v[42:43], 0\n"
        "s_waitcnt lgkmcnt(0)\n"
        "s_cmp_eq_u32 s72, 62\n"
        "s_waitcnt vmcnt(1)\n"
        "LR_retB:\n"
        "v_mfma_f32_16x16x32_bf16 v[96:99], v[60:63], v[120:123], 0\n"
        "s_waitcnt vmcnt(0)\n"
        "v_max3_u32 v100, v120, v121, v122\n"
        "v_mfma_f32_16x16x32_bf16 v[96:99], v[60:63], v[124:127], v[96:99]\n"
        "v_max3_u32 v101, v123, v124, v125\n"
        "v_max3_u32 v100, v100, v126, v127\n"
        "v_max_u32 v100, v100, v101\n"
        "v_cmp_eq_u32 vcc, -1, v100\n"
        "s_cbranch_vccnz LR_slowB\n"
        "s_nop 2\n"
        "v_add_f32_dpp v96, v96, v96 row_ror:4 row_mask:0xf bank_mask:0xf\n"
        "v_add_f32_dpp v97, v97, v97 row_ror:4 row_mask:0xf bank_mask:0xf\n"
        "v_add_f32_dpp v98, v98, v98 row_ror:4 row_mask:0xf bank_mask:0xf\n"
        "v_add_f32_dpp v99, v99, v99 row_ror:4 row_mask:0xf bank_mask:0xf\n"
        "v_add_f32_dpp v96, v96, v96 row_ror:8 row_mask:0xf bank_mask:0xf\n"
        "v_add_f32_dpp v97, v97, v97 row_ror:8 row_mask:0xf bank_mask:0xf\n"
        "v_add_f32_dpp v98, v98, v98 row_ror:8 row_mask:0xf bank_mask:0xf\n"
        "v_add_f32_dpp v99, v99, v99 row_ror:8 row_mask:0xf bank_mask:0xf\n"
        "v_cvt_pk_bf16_f32 v102, v96, v97\n"
        "v_cvt_pk_bf16_f32 v103, v98, v99\n"
        "buffer_store_dwordx2 v[102:103], v13, s[40:43], s34 offen sc1\n"
        "s_cbranch_scc1 LR_done\n"
        "s_barrier\n"
        "ds_read_b64 v[96:97], v9 offset:17408\n"
        "s_memrealtime s[88:89]\n"
        "s_nop 2\n"
        "s_memrealtime s[54:55]\n"
        "s_nop 2\n"
        "s_memrealtime s[56:57]\n"
        "s_nop 2\n"
        "s_memrealtime s[58:59]\n"
        "s_nop 2\n"
        "s_memrealtime s[98:99]\n"
        "s_nop 2\n"
        "s_memrealtime s[48:49]\n"
        "s_waitcnt lgkmcnt(0)\n"
        "v_readfirstlane_b32 s68, v96\n"
        "v_readfirstlane_b32 s52, v97\n"
        "s_add_u32 s88, s88, s54\n"
        "s_add_u32 s56, s56, s58\n"
        "s_add_u32 s98, s98, s48\n"
        "s_add_u32 s88, s88, s56\n"
        "s_add_u32 s88, s88, s98\n"
        "s_sub_u32 s88, s88, s97\n"
        "s_lshr_b32 s74, s88, 2\n"
        "s_sub_i32 s66, s68, s74\n"
        "s_cmp_ge_i32 s66, 0\n"
        "s_cselect_b32 s66, s66, 28\n"
        "s_max_i32 s66, s66, 12\n"
        "s_min_i32 s66, s66, 203\n"
        "s_lshl_b32 s66, s66, 2\n"
        "s_sub_u32 s48, s46, s66\n"
        "s_subb_u32 s49, s47, 0\n"
        "s_setpc_b64 s[48:49]\n"
        "s_nop 3\n"
        "s_nop 3\n"
        "s_nop 3\n"
        "s_nop 3\n"
        "s_nop 3\n"
        "s_nop 3\n"
        "s_nop 3\n"
        "s_nop 3\n"
        "s_nop 3\n"
        "s_nop 3\n"
        "s_nop 3\n"
        "s_nop 3\n"
        "s_nop 3\n"
        "s_nop 3\n"
        "s_nop 3\n"
        "s_nop 3\n"
        "s_nop 3\n"
        "s_nop 3\n"
        "s_nop 3\n"
        "s_nop 3\n"
        "s_nop 3\n"
        "s_nop 3\n"
        "s_nop 3\n"
        "s_nop 3\n"
        "s_nop 3\n"
        "s_nop 3\n"
        "s_nop 3\n"
        "s_nop 3\n"
        "s_nop 3\n"
        "s_nop 3\n"
        "s_nop 3\n"
        "s_nop 3\n"
        "s_nop 3\n"
        "s_nop 3\n"
        "s_nop 3\n"
        "s_nop 3\n"
        "s_nop 3\n"
        "s_nop 3\n"
        "s_nop 3\n"
        "s_nop 3\n"
        "s_nop 3\n"
        "s_nop 3\n"
        "s_nop 3\n"
        "s_nop 3\n"
        "s_nop 3\n"
        "s_nop 3\n"
        "s_nop 3\n"
        "s_nop 3\n"
        "s_nop 3\n"
        "s_nop 3\n"
        "s_nop 3\n"
        "s_nop 3\n"
        "s_nop 3\n"
        "s_nop 3\n"
        "s_nop 3\n"
        "s_nop 3\n"
        "s_nop 3\n"
        "s_nop 3\n"
        "s_nop 3\n"
        "s_nop 3\n"
        "s_nop 3\n"
        "s_nop 3\n"
        "s_nop 3\n"
        "s_nop 3\n"
        "s_nop 3\n"
        "s_nop 3\n"
        "s_nop 3\n"
        "s_nop 3\n"
        "s_nop 3\n"
        "s_nop 3\n"
        "s_nop 3\n"
        "s_nop 3\n"
        "s_nop 3\n"
        "s_nop 3\n"
        "s_nop 3\n"
        "s_nop 3\n"
        "s_nop 3\n"
        "s_nop 3\n"
        "s_nop 3\n"
        "s_nop 3\n"
        "s_nop 3\n"
        "s_nop 3\n"
        "s_nop 3\n"
        "s_nop 3\n"
        "s_nop 3\n"
        "s_nop 3\n"
        "s_nop 3\n"
        "s_nop 3\n"
        "s_nop 3\n"
        "s_nop 3\n"
        "s_nop 3\n"
        "s_nop 3\n"
        "s_nop 3\n"
        "s_nop 3\n"
        "s_nop 3\n"
        "s_nop 3\n"
        "s_nop 3\n"
        "s_nop 3\n"
        "s_nop 3\n"
        "s_nop 3\n"
        "s_nop 3\n"
        "s_nop 3\n"
        "s_nop 3\n"
        "s_nop 3\n"
        "s_nop 3\n"
        "s_nop 3\n"
        "s_nop 3\n"
        "s_nop 3\n"
        "s_nop 3\n"
        "s_nop 3\n"
        "s_nop 3\n"
        "s_nop 3\n"
        "s_nop 3\n"
        "s_nop 3\n"
        "s_nop 3\n"
        "s_nop 3\n"
        "s_nop 3\n"
        "s_nop 3\n"
        "s_nop 3\n"
        "s_nop 3\n"
        "s_nop 3\n"
        "s_nop 3\n"
        "s_nop 3\n"
        "s_nop 3\n"
        "s_nop 3\n"
        "s_nop 3\n"
        "s_nop 3\n"
        "s_nop 3\n"
        "s_nop 3\n"
        "s_nop 3\n"
        "s_nop 3\n"
        "s_nop 3\n"
        "s_nop 3\n"
        "s_nop 3\n"
        "s_nop 3\n"
        "s_nop 3\n"
        "s_nop 3\n"
        "s_nop 3\n"
        "s_nop 3\n"
        "s_nop 3\n"
        "s_nop 3\n"
        "s_nop 3\n"
        "s_nop 3\n"
        "s_nop 3\n"
        "s_nop 3\n"
        "s_nop 3\n"
        "s_nop 3\n"
        "s_nop 3\n"
        "s_nop 3\n"
        "s_nop 3\n"
        "s_nop 3\n"
        "s_nop 3\n"
        "s_nop 3\n"
        "s_nop 3\n"
        "s_nop 3\n"
        "s_nop 3\n"
        "s_nop 3\n"
        "s_nop 3\n"
        "s_nop 3\n"
        "s_nop 3\n"
        "s_nop 3\n"
        "s_nop 3\n"
        "s_nop 3\n"
        "s_nop 3\n"
        "s_nop 3\n"
        "s_nop 3\n"
        "s_nop 3\n"
        "s_nop 3\n"
        "s_nop 3\n"
        "s_nop 3\n"
        "s_nop 3\n"
        "s_nop 3\n"
        "s_nop 3\n"
        "s_nop 3\n"
        "s_nop 3\n"
        "s_nop 3\n"
        "s_nop 3\n"
        "s_nop 3\n"
        "s_nop 3\n"
        "s_nop 3\n"
        "s_nop 3\n"
        "s_nop 3\n"
        "s_nop 3\n"
        "s_nop 3\n"
        "s_nop 3\n"
        "s_nop 3\n"
        "s_nop 3\n"
        "s_nop 3\n"
        "s_nop 3\n"
        "s_nop 3\n"
        "s_nop 3\n"
        "s_nop 3\n"
        "LR_slA:\n"
        "buffer_load_dwordx4 v[96:99], v1, s[40:43], s76 offen sc1\n"
        "buffer_load_dwordx4 v[100:103], v1, s[40:43], s76 offen offset:1024 sc1\n"
        "v_mov_b64 v[60:61], -1\n"
        "v_mov_b64 v[62:63], -1\n"
        "s_add_u32 s72, s72, 1\n"
        "s_add_u32 s96, s72, 2\n"
        "s_nop 15\n"
        "s_nop 13\n"
        "buffer_load_dword v19, off, s[40:43], s62 sc1\n"
        "buffer_store_dwordx4 v[60:63], v12, s[40:43], s73 offen sc1\n"
        "buffer_store_dwordx4 v[60:63], v12, s[40:43], s73 offen offset:1024 sc1\n"
        "s_lshr_b32 s66, s72, 3\n"
        "s_and_b32 s66, s66, 2\n"
        "s_lshr_b32 s66, s35, s66\n"
        "s_lshr_b32 s67, s72, 1\n"
        "s_and_b32 s67, s67, 2\n"
        "s_xor_b32 s66, s66, s67\n"
        "s_xor_b32 s66, s66, 2\n"
        "s_and_b32 s66, s66, 3\n"
        "s_lshl_b32 s66, s66, 13\n"
        "s_lshl_b32 s95, s72, 15\n"
        "s_add_u32 s95, s95, s66\n"
        "s_xor_b32 s73, s73, 0x100000\n"
        "s_xor_b32 s75, s75, 0x100000\n"
        "s_branch LR_top\n"
        "LR_slowA:\n"
        "s_waitcnt lgkmcnt(0)\n"
        "buffer_load_dwordx4 v[96:99], v1, s[40:43], s76 offen sc1\n"
        "buffer_load_dwordx4 v[100:103], v1, s[40:43], s76 offen offset:1024 sc1\n"
        "s_add_u32 s81, s81, 1\n"
        "s_cmp_gt_u32 s81, 0x40000\n"
        "s_cbranch_scc1 LR_abort\n"
        "s_waitcnt vmcnt(0)\n"
        "v_max3_u32 v60, v96, v97, v98\n"
        "v_max3_u32 v61, v99, v100, v101\n"
        "v_max3_u32 v60, v60, v102, v103\n"
        "v_max_u32 v60, v60, v61\n"
        "v_cmp_eq_u32 vcc, -1, v60\n"
        "s_cbranch_vccnz LR_slowA\n"
        "v_mov_b64 v[36:37], 0\n"
        "v_mov_b64 v[38:39], 0\n"
        "v_mov_b64 v[40:41], 0\n"
        "v_mov_b64 v[42:43], 0\n"
        "v_mov_b64 v[44:45], 0\n"
        "v_mov_b64 v[46:47], 0\n"
        "v_mov_b64 v[48:49], 0\n"
        "v_mov_b64 v[50:51], 0\n"
        "s_nop 1\n"
        "s_branch LR_retA\n"
        "LR_slowB:\n"
        "LR_slowB2:\n"
        "buffer_load_dwordx4 v[120:123], v12, s[40:43], s73 offen sc1\n"
        "buffer_load_dwordx4 v[124:127], v12, s[40:43], s73 offen offset:1024 sc1\n"
        "s_add_u32 s81, s81, 1\n"
        "s_cmp_gt_u32 s81, 0x40000\n"
        "s_cbranch_scc1 LR_abort\n"
        "s_waitcnt vmcnt(0)\n"
        "v_max3_u32 v100, v120, v121, v122\n"
        "v_max3_u32 v101, v123, v124, v125\n"
        "v_max3_u32 v100, v100, v126, v127\n"
        "v_max_u32 v100, v100, v101\n"
        "v_cmp_eq_u32 vcc, -1, v100\n"
        "s_cbranch_vccnz LR_slowB2\n"
        "s_cmp_eq_u32 s72, 62\n"
        "s_branch LR_retB\n"
        "LR_slowKV:\n"
        "buffer_load_dword v19, off, s[40:43], s62 sc1\n"
        "s_add_u32 s81, s81, 1\n"
        "s_cmp_gt_u32 s81, 0x40000\n"
        "s_cbranch_scc1 LR_abort\n"
        "s_waitcnt vmcnt(0)\n"
        "v_readfirstlane_b32 s80, v19\n"
        "s_sleep 1\n"
        "s_add_u32 s66, s72, 2\n"
        "s_cmp_lt_u32 s80, s66\n"
        "s_cbranch_scc1 LR_slowKV\n"
        "s_branch LR_retKV\n"
        "LR_slowS:\n"
        "s_mov_b64 exec, -1\n"
        "s_nop 9\n"
        "v_max3_f32 v40, v36, v37, v38\n"
        "v_max3_f32 v40, v40, v39, v44\n"
        "v_max_f32 v40, v40, v45\n"
        "s_nop 1\n"
        "v_max_f32_dpp v40, v40, v40 row_ror:8 row_mask:0xf bank_mask:0xf\n"
        "v_mov_b32 v41, v40\n"
        "s_nop 1\n"
        "v_permlane16_swap_b32 v40, v41\n"
        "v_max_f32 v40, v40, v41\n"
        "v_mov_b32 v41, v40\n"
        "s_nop 1\n"
        "v_permlane32_swap_b32 v40, v41\n"
        "v_max_f32 v40, v40, v41\n"
        "v_sub_f32 v36, v36, v40\n"
        "v_sub_f32 v37, v37, v40\n"
        "v_sub_f32 v38, v38, v40\n"
        "v_sub_f32 v39, v39, v40\n"
        "v_sub_f32 v44, v44, v40\n"
        "v_sub_f32 v45, v45, v40\n"
        "v_mul_f32 v104, s86, v36\n"
        "v_mul_f32 v105, s86, v37\n"
        "v_mul_f32 v106, s86, v38\n"
        "v_mul_f32 v107, s86, v39\n"
        "v_exp_f32 v104, v104\n"
        "v_exp_f32 v105, v105\n"
        "v_exp_f32 v106, v106\n"
        "v_exp_f32 v107, v107\n"
        "s_nop 0\n"
        "v_cvt_pk_bf16_f32 v60, v104, v105\n"
        "v_cvt_pk_bf16_f32 v61, v106, v107\n"
        "s_nop 1\n"
        "v_mov_b32_dpp v62, v60 row_ror:8 row_mask:0xf bank_mask:0xf\n"
        "v_mov_b32_dpp v63, v61 row_ror:8 row_mask:0xf bank_mask:0xf\n"
        "s_nop 1\n"
        "s_branch LR_retS\n"
        "LR_restart:\n"
        "s_waitcnt vmcnt(0) lgkmcnt(0)\n"
        "s_mov_b64 s[0:1], s[98:99]\n"
        "s_or_b32 s2, s63, 0x10000\n"
        "s_lshl_b32 s3, s61, 6\n"
        "v_mbcnt_lo_u32_b32 v0, -1, 0\n"
        "v_mbcnt_hi_u32_b32 v0, -1, v0\n"
        "v_add_u32 v0, s3, v0\n"
        "s_getpc_b64 s[4:5]\n"
        "LR_rs1:\n"
        "s_add_u32 s4, s4, _Z6k_main6Params-LR_rs1\n"
        "s_addc_u32 s5, s5, -1\n"
        "s_setpc_b64 s[4:5]\n"
        "LR_abort:\n"
        "v_mov_b32 v60, 1\n"
        "s_mov_b32 s66, 4210688\n"
        "buffer_store_dword v60, off, s[40:43], s66 sc1\n"
        "LR_done:\n"
        "s_waitcnt vmcnt(0) lgkmcnt(0)\n"
        "LR_end:\n"
        "s_endpgm\n"
        :
        : "{v[20:27]}"(vin2), "{s[44:47]}"(rq), "{s[48:51]}"(rk), "{s[52:55]}"(rv), "{s[56:59]}"(ro), "{s60}"(s_wl), "{v[0:15]}"(vin0), "{v[16:17]}"(vin1), "{s[40:43]}"(ws),
          "{s61}"(w), "{s62}"(s_kvf), "{s39}"(wg), "{s63}"(bid), "{s97}"(xcc), "{s[98:99]}"(karg)
        : "v255", "a255", "vcc", "scc", "memory");
    __builtin_amdgcn_endpgm();
}

DEV void ln8(u32x4 (&xv)[8]) {
    float sum = 0.f;
#pragma unroll
    for (int j = 0; j < 8; ++j)
#pragma unroll
        for (int q = 0; q < 4; ++q) sum += bflo(xv[j][q]) + bfhi(xv[j][q]);
    sum += ror_f<8>(sum); sum = add_x16(sum); sum = add_x32(sum);
    const float mean = sum * (1.0f / 512.0f);
    float sq = 0.f;
#pragma unroll
    for (int j = 0; j < 8; ++j)
#pragma unroll
        for (int q = 0; q < 4; ++q) { const float d0 = bflo(xv[j][q]) - mean, d1 = bfhi(xv[j][q]) - mean; sq = fmaf(d0, d0, sq); sq = fmaf(d1, d1, sq); }
    sq += ror_f<8>(sq); sq = add_x16(sq); sq = add_x32(sq);
    const float mb = bfr(mean), rden = 1.0f / bfr(sqrtf(bfr(bfr(sq * (1.0f / 512.0f)) + 1e-5f)));
#pragma unroll
    for (int j = 0; j < 8; ++j)
#pragma unroll
        for (int q = 0; q < 4; ++q) {
            const unsigned d = pk(bflo(xv[j][q]) - mb, bfhi(xv[j][q]) - mb);
            xv[j][q] = pk(bflo(d) * rden, bfhi(d) * rden);
        }
}

#define DSR2(A0, A1, KS) asm volatile("ds_read_b128 %0, %2 offset:%3\n\tds_read_b128 %1, %2 offset:%4" : "=&v"(A0), "=&v"(A1) : "v"(X), "i"((KS) * 1024), "i"(16384 + (KS) * 1024) : "memory")
template <int MODE>
DEV void proj_loop(const Params& p, rsrc_t ws, LAS unsigned char* lds, int w, const unsigned short* wsrc, unsigned ldw, unsigned wbytes, int col0,
                   unsigned obase, unsigned ostride, unsigned flag, int s0, int sstep, Hs& hs, unsigned a0_, unsigned xcc) {
    const int lane = lane_id(), c = lane & 15, g = lane >> 4; const bool lead = (w == 0) && (lane == 0);
    TLS(0);
    u32x4 W[4][16], x0[16];
    unsigned xf = 0;
    {
        const rsrc_t r = mk_rsrc(wsrc, wbytes);
        const int gam = (MODE == 1) ? c : (8 * ((c >> 1) & 1) + 2 * (c >> 2) + (c & 1));
        const unsigned voff = (unsigned)(8 * g) * ldw + (unsigned)(col0 + 4 * gam) * 2u;
        u32x2 L[16][8];
        auto fetch = [&](int ks) {
#pragma unroll
            for (int j = 0; j < 8; ++j) L[ks][j] = __builtin_amdgcn_raw_buffer_load_b64(r, voff, (unsigned)(32 * ks + j) * ldw, PROJ_AUX);
        };
        fetch(0); fetch(1);
        asm volatile("" ::: "memory");
        hs_send(ws, hs, a0_, xcc);
#pragma unroll
        for (int ks = 2; ks < 7; ++ks) fetch(ks);
#pragma unroll
        for (int ks = 0; ks < 16; ++ks) {
#pragma unroll
            for (int t = 0; t < 4; ++t) {
#pragma unroll
                for (int m = 0; m < 4; ++m) W[t][ks][m] = (t & 1) ? phi(L[ks][2 * m][t >> 1], L[ks][2 * m + 1][t >> 1]) : plo(L[ks][2 * m][t >> 1], L[ks][2 * m + 1][t >> 1]);
                if (t < 3) PIN_A(W[t][ks]); else PIN_V(W[t][ks]);
            }
            asm volatile("" ::: "memory");
            if (ks + 7 < 16) fetch(ks + 7);
            if (ks < 3 && !hs.again) stflag(hs.req, ws, FL_REQ + hs.slot);
            if (ks == XF_AT) hs_wait(ws, hs);
            if (ks == XF_AT) xf = ldflag(ws, FL_XN + (unsigned)(s0 + (lane & 1) * sstep) * 128u);
            if (ks == XF_AT + 7) {
                if (!__all(xf != 0u)) wait_flags(ws, FL_XN + (unsigned)(s0 + (lane & 1) * sstep) * 128u, 1u, 0);
#pragma unroll
                for (int i = 0; i < 16; ++i) x0[i] = ld16(ws, (unsigned)lane * 16u, OFF_XN + (unsigned)(s0 + (i >> 3) * sstep) * 32768u + (unsigned)w * 8192u + (unsigned)(i & 7) * 1024u);
            }
            asm volatile("" ::: "memory");
        }
    }
    xf = ldflag(ws, FL_XN + (unsigned)lane * 128u);
#pragma unroll
    for (int i = 0; i < 8; ++i) lds_w16(lds + w * 8192 + i * 1024 + lane * 16, x0[i]);
    WG_BARRIER();
    TLS(1);
    int buf = 0;
    u32x4 xr[8], pend[4];
#pragma unroll
    for (int i = 0; i < 8; ++i) xr[i] = x0[8 + i];
#pragma unroll
    for (int i = 0; i < 4; ++i) pend[i] = xr[i];
    const unsigned vst = (unsigned)lane * 16u;
    unsigned vpend = 0x7FFFFF00u;
    u32x4 a0[4], a1[4];
    { LAS unsigned char* X = lds + lane * 16; DSR2(a0[0], a1[0], 0); DSR2(a0[1], a1[1], 1); }
    asm volatile("s_waitcnt lgkmcnt(0)" : "+v"(a0[0]), "+v"(a1[0]), "+v"(a0[1]), "+v"(a1[1]) :: "memory");
    for (int s = s0; s < 64; s += sstep) {
        if (s == s0 + sstep) TLS(2);
        LAS unsigned char* X = lds + buf * 32768 + lane * 16;
        LAS unsigned char* Y = lds + (buf == 2 ? 0 : buf + 1) * 32768 + w * 8192 + lane * 16;
        const unsigned pb = obase + (unsigned)(s - sstep) * 2u * ostride;
        const unsigned xb = OFF_XN + (unsigned)min(s + 2 * sstep, 63) * 32768u + (unsigned)w * 8192u;
        f32x4 acc[4][2];
#pragma unroll
        for (int t = 0; t < 4; ++t) { acc[t][0] = (f32x4){0.f, 0.f, 0.f, 0.f}; acc[t][1] = acc[t][0]; }
        LAS unsigned char* XN_ = lds + (buf == 2 ? 0 : buf + 1) * 32768 + lane * 16;
#pragma unroll
        for (int ks = 0; ks < 16; ++ks) {
            if (ks == 6 && s > s0 && s <= s0 + 6 * sstep) {
                asm volatile("s_waitcnt vmcnt(0)\n\ts_barrier" ::: "memory");
                if (lead) stflag((unsigned)(s - sstep + 1), ws, flag);
            }
            if (ks == 12) {
                asm volatile("s_waitcnt vmcnt(0)\n\ts_barrier" ::: "memory");
                if (s == s0 && !__all(xf != 0u)) wait_flags(ws, FL_XN + (unsigned)lane * 128u, 1u, 0);
            }
            asm volatile("s_waitcnt lgkmcnt(2)" : "+v"(a0[ks & 3]), "+v"(a1[ks & 3]) :: "memory");
#pragma unroll
            for (int t = 0; t < 4; ++t) {
                if (MODE == 1) { acc[t][0] = mf32(a0[ks & 3], W[t][ks], acc[t][0]); acc[t][1] = mf32(a1[ks & 3], W[t][ks], acc[t][1]); }
                else { acc[t][0] = mf32(W[t][ks], a0[ks & 3], acc[t][0]); acc[t][1] = mf32(W[t][ks], a1[ks & 3], acc[t][1]); }
                if (ks == 15 && t > 0) continue;
                FENCE();
                if (t == 0) {
                    if (ks < 4) st16(pend[ks], ws, vpend + (MODE == 1 ? (unsigned)ks * 1024u : (unsigned)(ks & 1) * 1024u), pb + (MODE == 1 ? 0u : (unsigned)(ks >> 1) * ostride));
                    if (ks >= 12) xr[2 * (ks - 12)] = ld16(ws, vst, xb + (unsigned)(2 * (ks - 12)) * 1024u);
                }
                if (t == 1 && ks >= 2 && ks < 10) asm volatile("ds_write_b128 %0, %1 offset:%2" :: "v"(Y), "v"(xr[ks - 2]), "i"(((ks - 2) & 7) * 1024) : "memory");
                if (t == 2 || ks == 15) {
                    switch (ks) {
#define C_(K) case K: DSR2(a0[(K + 2) & 3], a1[(K + 2) & 3], K + 2); break;
                        C_(0) C_(1) C_(2) C_(3) C_(4) C_(5) C_(6) C_(7) C_(8) C_(9) C_(10) C_(11) C_(12) C_(13)
#undef C_
                        case 14: { LAS unsigned char* X = XN_; DSR2(a0[0], a1[0], 0); } break;
                        default: { LAS unsigned char* X = XN_; DSR2(a0[1], a1[1], 1); } break;
                    }
                    if (ks >= 12) xr[2 * (ks - 12) + 1] = ld16(ws, vst, xb + (unsigned)(2 * (ks - 12) + 1) * 1024u);
                }
                if (ks == 15) { asm volatile("" ::: "memory"); stflag((unsigned)(s - sstep + 1), ws, (lead && s > s0) ? flag : 0x7FFFFF00u); }
                FENCE();
            }
        }
        if (MODE == 1) {
#pragma unroll
            for (int t = 0; t < 4; ++t) {
                pend[t][0] = pk(acc[t][0][0], acc[t][0][1]); pend[t][1] = pk(acc[t][0][2], acc[t][0][3]); pend[t][2] = pk(acc[t][1][0], acc[t][1][1]); pend[t][3] = pk(acc[t][1][2], acc[t][1][3]);
            }
        } else {
            if (MODE == 2) {
#pragma unroll
                for (int t = 0; t < 4; ++t)
#pragma unroll
                    for (int T = 0; T < 2; ++T)
#pragma unroll
                        for (int r = 0; r < 4; ++r) {
                            const float hv = bfr(acc[t][T][r]);
                            acc[t][T][r] = hv * __builtin_amdgcn_rcpf(1.0f + __builtin_amdgcn_exp2f(-1.4426950408889634f * hv));
                        }
            }
#pragma unroll
            for (int T = 0; T < 2; ++T)
#pragma unroll
                for (int a = 0; a < 2; ++a) {
                    u32x4& v = pend[2 * T + a];
                    v[0] = pk(acc[0][T][2 * a], acc[1][T][2 * a]); v[1] = pk(acc[2][T][2 * a], acc[3][T][2 * a]);
                    v[2] = pk(acc[0][T][2 * a + 1], acc[1][T][2 * a + 1]); v[3] = pk(acc[2][T][2 * a + 1], acc[3][T][2 * a + 1]);
                }
        }
        asm volatile("s_waitcnt lgkmcnt(0)" : "+v"(a0[0]), "+v"(a1[0]), "+v"(a0[1]), "+v"(a1[1]), "+v"(pend[3]) :: "memory");
        vpend = vst;
        if (s <= s0 + 5 * sstep || s + sstep >= 64) {
            const unsigned cb = obase + (unsigned)s * 2u * ostride;
#pragma unroll
            for (int i = 0; i < 4; ++i) st16(pend[i], ws, vst + (MODE == 1 ? (unsigned)i * 1024u : (unsigned)(i & 1) * 1024u), cb + (MODE == 1 ? 0u : (unsigned)(i >> 1) * ostride));
            vpend = 0x7FFFFF00u;
        }
        buf = (buf == 2) ? 0 : buf + 1;
    }
    asm volatile("s_waitcnt vmcnt(0)\n\ts_barrier" ::: "memory");
    if (lead) stflag(64u, ws, flag);
    TLS(3);
}

DEV void role_PROJ(const Params& p, rsrc_t ws, LAS unsigned char* lds, int w, int id, Hs& hs, unsigned a0, unsigned xcc) {
    const unsigned flag = FL_PROJ + (unsigned)id * 128u;
    if (id >= 32) for (int i = 0; i < QH_SLEEP; ++i) __builtin_amdgcn_s_sleep(16);
    if (id < 32 && w >= 2) {
        proj_loop<1>(p, ws, lds, w, p.wv, 8192u, 512u * 8192u, id * 128 + 64 * (w - 2), OFF_VT + (unsigned)id * HSTRIDE + (unsigned)(w - 2) * 4096u, 4096u, flag, 0, 1, hs, a0, xcc);
    } else if (id < 48) {
        const bool isk = id < 32;
        const int col0 = isk ? id * 128 + 64 * w : (id - 32) * 256 + 64 * w;
        proj_loop<0>(p, ws, lds, w, isk ? p.wk : p.wq, 8192u, 512u * 8192u, col0, (isk ? OFF_K : OFF_Q) + (unsigned)(col0 >> 7) * HSTRIDE + (unsigned)((col0 >> 5) & 3) * 1024u, 4096u, flag, 0, 1, hs, a0, xcc);
    } else {
        const int col0 = ((id - 48) >> 1) * 256 + 64 * w;
        proj_loop<2>(p, ws, lds, w, p.w1, 3584u, 512u * 3584u, col0, OFF_H + (unsigned)(col0 >> 7) * HSTRIDE + (unsigned)((col0 >> 5) & 3) * 1024u, 4096u, flag, (id - 48) & 1, 2, hs, a0, xcc);
    }
}

DEV void role_MA(const Params& p, rsrc_t ws, LAS unsigned char* lds, int w, int h, int sb) {
    const int lane = lane_id(), c = lane & 15, g = lane >> 4; const bool lead = (w == 0) && (lane == 0);
    TLS(0);
    const int idx = (c & 1) ? 0xEEEE : 0x4444;
    const unsigned oflag = (w == 0 && lane < 2) ? FL_O + (unsigned)h * 128u + (unsigned)(2 * sb + lane) * 4096u : 0x7FFFFF00u;
    for (int i = 0; i < MA_SLEEP; ++i) __builtin_amdgcn_s_sleep(16);
    u32x8 WK[2][8], WV[2][8];
    {
        const unsigned voff = (unsigned)(32 * (g >> 1) + 8 * (g & 1)) * 8192u + (unsigned)(h * 128 + 32 * w + 2 * c) * 2u;
        const rsrc_t rk = mk_rsrc(p.wk, 512u * 4096u * 2u), rv = mk_rsrc(p.wv, 512u * 4096u * 2u);
#pragma unroll
        for (int u = 0; u < 8; ++u) {
            load_proj_frags(rk, voff, u, WK[0][u], WK[1][u]);
            load_proj_frags(rv, voff, u, WV[0][u], WV[1][u]);
            PIN_A(WK[0][u]); PIN_A(WK[1][u]); PIN_A(WV[0][u]); PIN_V(WV[1][u]);
        }
    }
    const unsigned vmemf = (unsigned)(2 * w) * 1024u + (unsigned)(16 * g + (c & 12) + 2 * (c & 1) + ((c >> 1) & 1)) * 16u;
    const unsigned vk = (unsigned)h * HSTRIDE + (unsigned)w * 1024u + (unsigned)lane * 16u;
    const unsigned vq = (unsigned)h * HSTRIDE + (unsigned)(2 * w) * 1024u + (unsigned)lane * 16u;
    LAS unsigned char* QR0 = lds + 32768 + (w >> 1) * 4096 + (16 * g + 8 * (w & 1) + (c & 7)) * 16;
    const unsigned vv = (unsigned)h * HSTRIDE + (unsigned)(2 * w) * 1024u + (unsigned)lane * 16u;
    const unsigned vo = (c < 8) ? (unsigned)h * HSTRIDE + (unsigned)(w >> 1) * 4096u + (unsigned)(g >> 1) * 1024u + (unsigned)(32 * (g & 1) + 8 * (w & 1) + c) * 16u : 0x7FFFFF00u;
    const unsigned pflag = FL_PROJ + (unsigned)((lane & 1) ? 32 + (h >> 1) : h) * 128u;
    const unsigned mflag = 4096u + (unsigned)h * 128u + 4u;
    const int tq = 8 * w + (c & 7), tk0 = 16 * (c >> 3) + 4 * g;
    const float SCALE = 0.08838834764831845f, L2E = 1.4426950408889634f;
    const f32x4 zero4 = {0.f, 0.f, 0.f, 0.f};
    LAS unsigned char* MX = lds;
    LAS unsigned char* KL = lds + 8192;
    LAS unsigned char* VL = lds + 12288;
    unsigned pj_ready = 0;
    LAS unsigned char* KV0 = lds + 16384 + lane * 16;
    u32x4 nq[2], nk[2], nv[2];
    auto load_qkv = [&](int s) {
        const unsigned so = (unsigned)s * 8192u;
        nk[0] = ld16(ws, vk, OFF_K + so); nk[1] = ld16(ws, vk, OFF_K + so + 4096u);
        nv[0] = ld16(ws, vv, OFF_VT + so); nv[1] = ld16(ws, vv + 1024u, OFF_VT + so);
        nq[0] = ld16(ws, vq, OFF_Q + so); nq[1] = ld16(ws, vq + 1024u, OFF_Q + so);
    };
    auto put_kv = [&](int s) {
        LAS unsigned char* KV = KV0 + ((s >> 1) & 1) * 24576;
        lds_w16(KV + w * 1024, nk[0]); lds_w16(KV + 4096 + w * 1024, nk[1]);
        lds_w16(KV + 8192 + 2 * w * 1024, nv[0]); lds_w16(KV + 8192 + (2 * w + 1) * 1024, nv[1]);
        lds_w16(KV + 16384 + 2 * w * 1024, nq[0]); lds_w16(KV + 16384 + (2 * w + 1) * 1024, nq[1]);
    };
    TLS(1);
    wait_flags(ws, FL_FILL + (unsigned)(lane & 31) * 128u, 1u, 0);
    wait_flags(ws, FL_CAL + 4u * (unsigned)(lane & 3), 1u, 0);
    const unsigned calv = ldflag(ws, FL_CAL + 4u * (unsigned)(lane & 3));
    const unsigned cal0 = (unsigned)__builtin_amdgcn_readlane(calv, 0) | (unsigned)__builtin_amdgcn_readlane(calv, 1);
    const unsigned cal = ((unsigned)__builtin_amdgcn_readlane(calv, 2) | (unsigned)__builtin_amdgcn_readlane(calv, 3)) ^ (((unsigned)h >> 3 & 1u) * 10u);
    auto mem_off = [&](int s) { return OFF_MEMF + (unsigned)s * 32768u + (s == 0 ? 0u : ((cal >> ((s >> 3) & 2)) & 3u) << 13); };
    wait_flags(ws, pflag, (unsigned)(sb + 1), 0);
    load_qkv(sb);
    put_kv(sb);
    unsigned pfl = ldflag(ws, pflag);
    WG_BARRIER();
    for (int s = sb; s < 64; s += 2) {
        if (s == sb + 2) TLS(2);
        if (s >= 62) TLS(4);
        u32x4 qf[4], vt[8];
        LAS unsigned char* KV = KV0 + ((s >> 1) & 1) * 24576;
        LAS unsigned char* QR = QR0 + ((s >> 1) & 1) * 24576;
        f32x4 sx;
        {
            u32x4 kf[2][4];
#pragma unroll
            for (int kb = 0; kb < 4; ++kb) { qf[kb] = lds_r16(QR + kb * 1024); kf[0][kb] = lds_r16(KV + kb * 1024); kf[1][kb] = lds_r16(KV + 4096 + kb * 1024); }
            f32x4 s0 = zero4, s1 = zero4;
#pragma unroll
            for (int kb = 0; kb < 4; ++kb) { s0 = mf32(kf[0][kb], qf[kb], s0); s1 = mf32(kf[1][kb], qf[kb], s1); }
#pragma unroll
            for (int r = 0; r < 4; ++r) { const float v = bfr(bfr((c < 8) ? s0[r] : s1[r]) * SCALE); sx[r] = (tk0 + r <= tq) ? v : -1e9f; }
        }
        PIN_V(sx);
        const int sn = s + 2;
        pj_ready = max(pj_ready, min((unsigned)__builtin_amdgcn_readlane(pfl, 0), (unsigned)__builtin_amdgcn_readlane(pfl, 1)));
        if (sn < 64 && pj_ready < (unsigned)(sn + 1)) { wait_flags(ws, pflag, (unsigned)(sn + 1), 0); pj_ready = (unsigned)(sn + 1); }
        const unsigned mo = mem_off(s);
        {
            unsigned spins = 0;
            if (s > 0) {
                const unsigned sh = (unsigned)min(s, 62), hint = mflag + sh * 32768u + ((((cal0 >> ((sh >> 3) & 2u)) ^ ((sh >> 1) & 2u) ^ 2u) & 3u) << 13);
                while ((unsigned)__builtin_amdgcn_readfirstlane(ldflag(ws, hint)) == SENT) { __builtin_amdgcn_s_sleep(MA_POLL); spin_check(spins, ws); }
                if (s == 63) __builtin_amdgcn_s_sleep(LAST_SLEEP);
            }
            for (;;) {
                asm volatile("" ::: "memory");
                const u32x4 g0 = ld16(ws, vmemf, mo), g1 = ld16(ws, vmemf + 1024u, mo);
                lds_w16(MX + (2 * w) * 1024 + lane * 16, g0); lds_w16(MX + (2 * w + 1) * 1024 + lane * 16, g1);
                if (__all(max(umax4(g0), umax4(g1)) != SENT)) break;
                __builtin_amdgcn_s_sleep(2);
                spin_check(spins, ws);
            }
        }
        WG_BARRIER();
        {
            u32x4 m[8];
#pragma unroll
            for (int u = 0; u < 8; ++u) m[u] = lds_r16(MX + u * 1024 + lane * 16);
            f32x4 ak0 = zacc(), ak1 = zacc(), av0 = zacc(), av1 = zacc();
            FENCE();
#pragma unroll
            for (int u = 0; u < 8; ++u) {
                ak0 = smf(m[u], WK[0][u], ak0, idx); ak1 = smf(m[u], WK[1][u], ak1, idx);
                av0 = smf(m[u], WV[0][u], av0, idx); av1 = smf(m[u], WV[1][u], av1, idx);
                PIN_V(av1); FENCE();
                vt[u] = lds_r16(KV + 8192 + u * 1024);
                asm volatile("" ::: "memory"); FENCE();
            }
            const unsigned ka = pk(ak0[0] + ak0[1], ak1[0] + ak1[1]), kb = pk(ak0[2] + ak0[3], ak1[2] + ak1[3]);
            LAS unsigned char* kw = KL + (w >> 1) * 1024 + (16 * (2 * (w & 1) + (c >> 3)) + 4 * g + (c & 1)) * 16 + 4 * ((c & 7) >> 1);
            *(LAS unsigned*)kw = ka; *(LAS unsigned*)(kw + 32) = kb;
            u32x2 vp; vp[0] = pk(av0[0] + av0[1], av0[2] + av0[3]); vp[1] = pk(av1[0] + av1[1], av1[2] + av1[3]);
            *(LAS u32x2*)(VL + ((w >> 1) * 256 + g * 64 + 32 * (w & 1) + 2 * c) * 4) = vp;
        }
        WG_BARRIER();
        const u32x4 km0 = lds_r16(KL + lane * 16), km1 = lds_r16(KL + 1024 + lane * 16);
        const u32x4 vm0 = lds_r16(VL + lane * 16), vm1 = lds_r16(VL + 1024 + lane * 16);
        f32x4 sm = zacc();
        sm = smf(km0, cat(qf[0], qf[1]), sm, idx); sm = smf(km1, cat(qf[2], qf[3]), sm, idx);
        const float smo = bfr(bfr((c < 8) ? sm[0] + sm[1] : sm[2] + sm[3]) * SCALE);
        float mx = fmaxf(fmaxf(fmaxf(sx[0], sx[1]), fmaxf(sx[2], sx[3])), smo);
        mx = fmaxf(mx, ror_f<8>(mx)); mx = max_x16(mx); mx = max_x32(mx);
        float e[5];
#pragma unroll
        for (int r = 0; r < 4; ++r) e[r] = __builtin_amdgcn_exp2f((sx[r] - mx) * L2E);
        e[4] = __builtin_amdgcn_exp2f((smo - mx) * L2E);
        float l = ((e[0] + e[1]) + (e[2] + e[3])) + e[4];
        l += ror_f<8>(l); l = add_x16(l); l = add_x32(l);
        const float ri = __builtin_amdgcn_rcpf(l);
        u32x4 pf; pf[0] = pk(e[0] * ri, e[1] * ri); pf[1] = pk(e[2] * ri, e[3] * ri); pf[2] = ror_u<8>(pf[0]); pf[3] = ror_u<8>(pf[1]);
        const float pmo = e[4] * ri;
        const u32x4 pm = {pk(pmo, ror_f<8>(pmo)), 0u, 0u, 0u};
        u32x4 ov[4];
#pragma unroll
        for (int wv = 0; wv < 2; ++wv) {
            f32x4 o[4];
#pragma unroll
            for (int t = 0; t < 4; ++t) {
                const unsigned vd = wv ? vm1[t] : vm0[t];
                const u32x4 va = {vd, vd, vd, vd};
                o[t] = mf32(vt[4 * wv + t], pf, zero4); o[t] = mf32(va, pm, o[t]);
            }
#pragma unroll
            for (int a = 0; a < 2; ++a) {
                u32x4& v = ov[2 * wv + a];
                v[0] = pk(o[0][2 * a], o[1][2 * a]); v[1] = pk(o[2][2 * a], o[3][2 * a]);
                v[2] = pk(o[0][2 * a + 1], o[1][2 * a + 1]); v[3] = pk(o[2][2 * a + 1], o[3][2 * a + 1]);
            }
        }
        asm volatile("" : "+v"(ov[3]) :: "memory");
        if (s < 62) { load_qkv(sn); pfl = ldflag(ws, pflag); }
#pragma unroll
        for (int i = 0; i < 4; ++i) st16(ov[i], ws, vo + (unsigned)(i >> 1) * 2048u + (unsigned)(i & 1) * 256u, OFF_O + (unsigned)s * 8192u);
        if (s < 62) put_kv(s + 2);
        DRAIN();
        WG_BARRIER();
        stflag((unsigned)(s + 1), ws, oflag);
    }
    TLS(3);
}

DEV void role_OUT(const Params& p, rsrc_t ws, LAS unsigned char* lds, int w, int set, int n) {
    const int lane = lane_id(), c = lane & 15, g = lane >> 4;
    TLS(0);
    for (int i = 0; i < OUT_SLEEP; ++i) __builtin_amdgcn_s_sleep(16);
    u32x4 WA[2][32], WB[2][14];
    {
        const rsrc_t ro = mk_rsrc(p.wo, 4096u * 1024u), r2 = mk_rsrc(p.w2, 1792u * 1024u);
        const unsigned voff = (unsigned)(8 * g) * 1024u + (unsigned)(32 * n + 2 * c) * 2u;
        unsigned L[46][8];
        auto fetch = [&](int ks) {
#pragma unroll
            for (int j = 0; j < 8; ++j)
                L[ks][j] = (ks < 32) ? __builtin_amdgcn_raw_buffer_load_b32(ro, voff, (unsigned)(1024 * w + 32 * ks + j) * 1024u, 0)
                                     : __builtin_amdgcn_raw_buffer_load_b32(r2, voff, (unsigned)(448 * w + 32 * (ks - 32) + j) * 1024u, 0);
        };
#pragma unroll
        for (int ks = 0; ks < 7; ++ks) fetch(ks);
#pragma unroll
        for (int ks = 0; ks < 46; ++ks) {
            u32x4 f0, f1;
#pragma unroll
            for (int m = 0; m < 4; ++m) { f0[m] = plo(L[ks][2 * m], L[ks][2 * m + 1]); f1[m] = phi(L[ks][2 * m], L[ks][2 * m + 1]); }
            if (ks < 30) { PIN_A(f0); PIN_A(f1); } else { PIN_V(f0); PIN_V(f1); }
            if (ks < 32) { WA[0][ks] = f0; WA[1][ks] = f1; } else { WB[0][ks - 32] = f0; WB[1][ks - 32] = f1; }
            asm volatile("" ::: "memory");
            if (ks + 7 < 46) fetch(ks + 7);
            asm volatile("" ::: "memory");
        }
    }
    const f32x4 zero4 = {0.f, 0.f, 0.f, 0.f};
    const unsigned vl = (unsigned)lane * 16u;
    const unsigned oflag = FL_O + (unsigned)set * 4096u + (unsigned)(8 * w + (lane & 7)) * 128u;
    const unsigned hflag = FL_PROJ + (unsigned)(48 + min(lane, 13)) * 128u;
    unsigned fo = 0, fh = 0;
    TLS(1);
    for (int k = -1, T = set - NSET; T < 128; ++k, T += NSET) {
        if (k == 1) TLS(2);
        const int s = T >> 1, Tn = T + NSET;
        asm volatile("" : "+v"(fo), "+v"(fh) :: "memory");
        if (Tn < 128 && !__all(fh >= (unsigned)((Tn >> 1) + 1) || ((min(lane, 13) ^ (Tn >> 1)) & 1)))
            wait_flags(ws, FL_PROJ + (unsigned)(48 + 2 * min(lane, 6) + ((Tn >> 1) & 1)) * 128u, (unsigned)((Tn >> 1) + 1), 1);
        const unsigned hb = Tn < 128 ? OFF_H + (unsigned)Tn * 4096u : 0x7F000000u;
        const unsigned ob = T >= 0 ? OFF_O + (unsigned)T * 4096u + (unsigned)(8 * w) * HSTRIDE : 0x7F000000u;
        u32x4 f[46];
#pragma unroll
        for (int i = 0; i < 14; ++i) f[i] = ld16(ws, vl, hb + (unsigned)((14 * w + i) >> 2) * HSTRIDE + (unsigned)((14 * w + i) & 3) * 1024u);
        const size_t oidx = (size_t)(16 * max(T, 0) + c) * 512u + (unsigned)(32 * n + 8 * g);
        const u32x4 xv = *(const u32x4*)(p.x + oidx);
        if (T >= 0 && !__all(fo >= (unsigned)(s + 1))) wait_flags(ws, oflag, (unsigned)(s + 1), s < 62 ? -1 : 0);
        if (Tn >= 128) TLS(4);
        asm volatile("" ::: "memory");
#pragma unroll
        for (int i = 14; i < 20; ++i) f[i] = ld16(ws, vl + ((i - 14) & 3) * 1024u, ob + ((i - 14) >> 2) * HSTRIDE);
        FENCE();
        f32x4 a0 = zero4, a1 = zero4, b0 = zero4, b1 = zero4;
#pragma unroll
        for (int i = 0; i < 46; ++i) {
            if (i < 14) { b0 = mf32(WB[0][i], f[i], b0); b1 = mf32(WB[1][i], f[i], b1); }
            else { a0 = mf32(WA[0][i - 14], f[i], a0); a1 = mf32(WA[1][i - 14], f[i], a1); }
            if (i + 20 < 46) { f[i + 20] = ld16(ws, vl + ((i + 6) & 3) * 1024u, ob + ((i + 6) >> 2) * HSTRIDE); FENCE(); }
            if (i + 20 == 46) { fo = ldflag(ws, oflag); fh = ldflag(ws, hflag); FENCE(); }
        }
        LAS unsigned char* RD = lds + (k & 1) * 8192;
        LAS unsigned char* RB = lds + 16384 + (k & 3) * 8192;
        LAS unsigned char* RN = lds + 16384 + ((k + 1) & 3) * 8192 + w * 2048 + lane * 16;
        lds_w16(RD + w * 2048 + lane * 16, __builtin_bit_cast(u32x4, a0)); lds_w16(RD + w * 2048 + 1024 + lane * 16, __builtin_bit_cast(u32x4, a1));
        lds_w16(RN, __builtin_bit_cast(u32x4, b0)); lds_w16(RN + 1024, __builtin_bit_cast(u32x4, b1));
        WG_BARRIER();
        if (T >= 0 && w == (k & 3)) {
            f32x4 A[2], B[2];
#pragma unroll
            for (int t = 0; t < 2; ++t) {
                A[t] = zero4; B[t] = zero4;
#pragma unroll
                for (int q = 0; q < 4; ++q) {
                    A[t] += __builtin_bit_cast(f32x4, lds_r16(RD + q * 2048 + t * 1024 + lane * 16));
                    B[t] += __builtin_bit_cast(f32x4, lds_r16(RB + q * 2048 + t * 1024 + lane * 16));
                }
            }
            u32x4 v;
#pragma unroll
            for (int r = 0; r < 4; ++r)
                v[r] = pk(bfr(bflo(xv[r]) + bfr(A[0][r])) + bfr(B[0][r]), bfr(bfhi(xv[r]) + bfr(A[1][r])) + bfr(B[1][r]));
            *(u32x4*)(p.out + oidx) = v;
        }
    }
    TLS(3);
}

DEV void start_job(const Params& p, rsrc_t ws, int w, int job, Hs& hs, unsigned a0, unsigned xcc) {
    const int lane = lane_id();
    if (job < 64) {
        const int row = 32 * job + 8 * w + (lane & 7), part = lane >> 3;
        u32x4 xv[8];
#pragma unroll
        for (int j = 0; j < 8; ++j) xv[j] = *(const u32x4*)(p.x + (size_t)row * 512u + (unsigned)(8 * j + part) * 8u);
        asm volatile("" ::: "memory");
        hs_send(ws, hs, a0, xcc);
        ln8(xv);
        const unsigned voff = (unsigned)(part >> 2) * 1024u + (unsigned)(16 * (part & 3) + (row & 15)) * 16u;
#pragma unroll
        for (int j = 0; j < 8; ++j) st16(xv[j], ws, voff, OFF_XN + (unsigned)(2 * job + (w >> 1)) * 16384u + (unsigned)j * 2048u);
    } else {
        const unsigned j = (unsigned)(job - 64), tid = (unsigned)(64 * w + lane);
        const u32x4 sent4 = {SENT, SENT, SENT, SENT};
        hs_send(ws, hs, a0, xcc);
        if (j == 16u) {
#pragma unroll
            for (unsigned k = 0; k < 2; ++k) {
                const unsigned ch = tid + 256u * k, ks = ch >> 6, ga = (ch >> 4) & 3u, gi = (ch >> 2) & 3u, half = (ch >> 1) & 1u, ri = ch & 1u;
                const unsigned short* src = p.mem0 + (2u * gi + ri) * 512u + 128u * (ks >> 1) + 4u * (ks & 1u) + 8u * (4u * ga + 2u * half);
                const u32x2 lo = *(const u32x2*)src, hi = *(const u32x2*)(src + 8);
                const u32x4 v = {min(lo[0], SENT - 1u), min(lo[1], SENT - 1u), min(hi[0], SENT - 1u), min(hi[1], SENT - 1u)};
                st16(v, ws, ch * 16u, OFF_MEMF);
            }
        }
        for (unsigned i = (j == 16u) ? 2u : 0u; i < 32u; ++i) st16(sent4, ws, tid * 16u, j * 131072u + i * 4096u);
    }
    hs_wait(ws, hs);
    DRAIN();
    WG_BARRIER();
    if (w == 0 && lane == 0) stflag(1u, ws, FL_XN + (unsigned)job * 128u);
}

DEV void role_CAL(rsrc_t ws, int id, const Hs& hs) {
    const int lane = lane_id();
    const u32x4 sent4 = {SENT, SENT, SENT, SENT};
    const unsigned voff = (lane < 32) ? (unsigned)(lane & 7) * 16u + (unsigned)(lane >> 3) * 2048u : 0x7FFFFF00u;
    unsigned best[4] = {~0u, ~0u, ~0u, ~0u};
    for (int rep = 0; rep < 4; ++rep) {
        if (!hs.again) { stflag(hs.req, ws, FL_REQ + hs.slot); DRAIN(); }
#pragma unroll
        for (int q = 0; q < 4; ++q) {
            unsigned long long t0, t1;
            asm volatile("s_memtime %0\n\ts_waitcnt lgkmcnt(0)" : "=s"(t0) :: "memory");
            st16(sent4, ws, voff, (unsigned)(id >> 1) * (2u << 20) + (unsigned)(1 + 16 * (id & 1)) * 32768u + (unsigned)q * 8192u);
            DRAIN();
            asm volatile("s_memtime %0\n\ts_waitcnt lgkmcnt(0)" : "=s"(t1) :: "memory");
            best[q] = min(best[q], (unsigned)(t1 - t0));
        }
    }
    unsigned bi = 0, bv = best[0];
#pragma unroll
    for (unsigned q = 1; q < 4; ++q) if (best[q] < bv) { bv = best[q]; bi = q; }
    hs_wait(ws, hs);
    if (lane == 0) stflag(0x100u | ((bi ^ 2u) << (2 * (id & 1))), ws, FL_CAL + 4u * (unsigned)id);
}

__global__ void __launch_bounds__(256, 1) k_main(Params p) {
    extern __shared__ __attribute__((aligned(16))) unsigned char smem[];
    LAS unsigned char* lds = (LAS unsigned char*)smem;
    const rsrc_t ws = mk_rsrc(p.ws, WS_BYTES);
    unsigned raw = blockIdx.x;
    asm volatile("" : "+s"(raw));
    const int b = (int)(raw & 255u), r = b >> 3, w = __builtin_amdgcn_readfirstlane(threadIdx.x >> 6);
    const unsigned xcc = ((unsigned)__builtin_amdgcn_s_getreg(20 | (3 << 11)) + XCC_OFF) & XCC_AND;
    Hs hs; hs.slot = (unsigned)b * 64u + (unsigned)w * 4u; hs.req = 0u; hs.bid = (unsigned)b; hs.w = w; hs.again = raw > 255u;
    if (hs.again) {
        if (lane_id() == 0) stflag(1u, ws, FL_QUIET + (unsigned)b * 16u + (unsigned)w * 4u);
        for (unsigned i = 0; i < 16u; ++i) wait_flags(ws, FL_QUIET + i * 256u + (unsigned)lane_id() * 4u, 1u, 0);
    }
    const int vx = hs.again ? (b & 7) : (int)xcc;
    if (vx < 4 && r < 16) { role_R(p, ws, (unsigned)w, (unsigned)(32 * (vx >> 1) + 16 * (r >> 3) + 8 * (vx & 1) + (r & 7)), raw, xcc); return; }
    unsigned a0 = ldflag(ws, FL_ACK + hs.slot);
    if (b == MASTER && !hs.again) {
        const unsigned keep = hs_master(ws, lds, w);
        hs_send(ws, hs, a0, xcc);
        stflag(hs.req & keep, ws, FL_ACK + hs.slot);
        hs_wait(ws, hs);
        WG_BARRIER();
    }
    if (vx < 4) {
        if (r >= 24) start_job(p, ws, w, (r - 24) * 4 + vx, hs, a0, xcc);
        else {
            hs_send(ws, hs, a0, xcc);
            if (vx == 1 && r < 20 && w == 0) role_CAL(ws, r - 16, hs);
            hs_wait(ws, hs);
        }
        role_MA(p, ws, lds, w, 8 * vx + (r & 7), (r >> 3) & 1);
        return;
    }
    if (r >= 16) { start_job(p, ws, w, 32 + (r - 16) * 4 + (vx - 4), hs, a0, xcc); role_OUT(p, ws, lds, w, vx - 4, r - 16); return; }
    const int j = vx - 4, q = r < 8 ? 8 * j + r : r < 12 ? 32 + 4 * j + (r - 8) : 48 + 4 * j + (r - 12);
    if (q < 62) role_PROJ(p, ws, lds, w, q, hs, a0, xcc);
    else { hs_send(ws, hs, a0, xcc); hs_wait(ws, hs); }
}

extern "C" void kernel_launch(void* const* d_in, const int* in_sizes, int n_in, void* d_out, int out_size, void* d_ws, size_t ws_size,
                              hipStream_t stream) {
    Params p;
    p.x = (const unsigned short*)d_in[0]; p.mem0 = (const unsigned short*)d_in[1]; p.wq = (const unsigned short*)d_in[2];
    p.wk = (const unsigned short*)d_in[3]; p.wv = (const unsigned short*)d_in[4]; p.wo = (const unsigned short*)d_in[5];
    p.w1 = (const unsigned short*)d_in[8]; p.w2 = (const unsigned short*)d_in[9];
    p.out = (unsigned short*)d_out;
    p.ws = (unsigned char*)(((size_t)d_ws + ((size_t)2 << 20) - 1) & ~(((size_t)2 << 20) - 1));
    (void)hipFuncSetAttribute((const void*)k_main, hipFuncAttributeMaxDynamicSharedMemorySize, LDS_BYTES);
    hipLaunchKernelGGL(k_main, dim3(256), dim3(256), LDS_BYTES, stream, p);
}
```
